```python
import torch

def _softmax(x, axis=-1):
    return torch.softmax(x, dim=axis)
def _gelu(x):
    return torch.nn.functional.gelu(x, approximate="tanh")
def _silu(x):
    return torch.nn.functional.silu(x)
def _sigmoid(x):
    return torch.sigmoid(x)
def _layernorm(x, axis=-1, eps=1e-5):
    m = x.mean(dim=axis, keepdim=True)
    v = x.var(dim=axis, keepdim=True, unbiased=False)
    return (x - m) / torch.sqrt(v + eps)
def _scatter_add(tgt, idx, src, axis=0):
    return tgt.clone().index_add_(axis, idx, src)
def _segment_sum(src, seg, n):
    out = torch.zeros((n,) + src.shape[1:], dtype=src.dtype)
    return out.index_add_(0, seg, src)
def _pad(x, pw):
    flat = tuple(v for pair in reversed(pw) for v in pair)
    return torch.nn.functional.pad(x, flat)

kernel_name = 'gated_mix__gqa_block_cached__glu_gated_glu__decode'

def setup_inputs():

    _w = (lambda a: (lambda t: t.to(torch.bfloat16) if t.is_floating_point() else t)(torch.as_tensor(a)))
    def _A_setup():
        B, Sq, D = 4, 1, 512
        Hq, Hkv, Sk, Dh = 32, 32, 4096, 128
        assert Hq % Hkv == 0, (Hq, Hkv)
        return {
            "x": _w((lambda *s: torch.randn(*s, dtype=torch.bfloat16) * 0.05)(B, Sq, D)),
            "k_cache": _w((lambda *s: torch.randn(*s, dtype=torch.bfloat16) * 6.4)(B, Hkv, Sk, Dh)),
            "v_cache": _w((lambda *s: torch.randn(*s, dtype=torch.bfloat16) * 0.4)(B, Hkv, Sk, Dh)),
            "wq": _w((lambda *s: torch.randn(*s, dtype=torch.bfloat16) * 6.4)(D, Hq * Dh) * (D ** -0.5)),
            "wo": _w((lambda *s: torch.randn(*s, dtype=torch.bfloat16) * 0.4)(Hq * Dh, D) * ((Hq * Dh) ** -0.5)),
        }
    _A_in = _A_setup()

    def _B_setup():
        B, S, D, F = 4, 1, 512, 1376
        return {
            "x": _w((lambda *s: torch.randn(*s, dtype=torch.bfloat16) * 0.05)(B, S, D)),
            "w_gate": _w((lambda *s: torch.randn(*s, dtype=torch.bfloat16) * 0.4)(D, F)),
            "w_up": _w((lambda *s: torch.randn(*s, dtype=torch.bfloat16) * 0.05)(D, F)),
            "w_down": _w((lambda *s: torch.randn(*s, dtype=torch.bfloat16) * 0.05)(F, D)),
        }
    _B_in = _B_setup()

    return {
        "A_x": _A_in["x"],
        "A_k_cache": _A_in["k_cache"],
        "A_v_cache": _A_in["v_cache"],
        "A_wq": _A_in["wq"],
        "A_wo": _A_in["wo"],
        "B_x": _B_in["x"],
        "B_w_gate": _B_in["w_gate"],
        "B_w_up": _B_in["w_up"],
        "B_w_down": _B_in["w_down"],
        "gate_w": _w((lambda *s: torch.randn(*s, dtype=torch.bfloat16) * 0.05)(512) * 16.0),
    }

def reference(A_x, A_k_cache, A_v_cache, A_wq, A_wo, B_x, B_w_gate, B_w_up, B_w_down, gate_w):
    def _A_ref(x, k_cache, v_cache, wq, wo):
        B, Sq, D = x.shape
        Hq, Dh = 32, 128
        Hkv = k_cache.shape[1]
        rep = Hq // Hkv
        q = (lambda x, *d: x.permute(*d))((x @ wq).reshape(B, Sq, Hq, Dh), 0, 2, 1, 3)
        k = (lambda x, n, axis: torch.repeat_interleave(x, n, dim=axis))(k_cache, rep, axis=1)
        v = (lambda x, n, axis: torch.repeat_interleave(x, n, dim=axis))(v_cache, rep, axis=1)
        scores = torch.einsum("bhqd,bhkd->bhqk", q, k) * (Dh ** -0.5)
        p = _softmax(scores, axis=-1)
        o = torch.einsum("bhqk,bhkd->bhqd", p, v)
        return (lambda x, *d: x.permute(*d))(o, 0, 2, 1, 3).reshape(B, Sq, Hq * Dh) @ wo

    def _B_ref(x, w_gate, w_up, w_down):
        g = torch.einsum("bsd,df->bsf", x, w_gate)
        u = torch.einsum("bsd,df->bsf", x, w_up)
        h = _sigmoid(g) * u
        return torch.einsum("bsf,fd->bsd", h, w_down)

    x = A_x
    g = _sigmoid((x * gate_w).sum(-1, keepdims=True))
    a = _A_ref(x, A_k_cache, A_v_cache, A_wq, A_wo)
    b = _B_ref(x, B_w_gate, B_w_up, B_w_down)
    return g * a + (1 - g) * b
```

```cpp
#include <hip/hip_runtime.h>
#include <hip/hip_bf16.h>
#include <stdint.h>

typedef unsigned u32x4 __attribute__((ext_vector_type(4)));
typedef float f32x2 __attribute__((ext_vector_type(2)));
typedef float f32x4 __attribute__((ext_vector_type(4)));
typedef __bf16 bf16x2 __attribute__((ext_vector_type(2)));
#define GAS __attribute__((address_space(1)))
typedef GAS unsigned gu32;
typedef GAS unsigned long long gu64;
#define RLX_AGENT __ATOMIC_RELAXED, __HIP_MEMORY_SCOPE_AGENT

namespace {

constexpr int DM = 512, NH = 32, SK = 4096, DH = 128, FF = 1376;
constexpr int NWG = 256, NWAVE = 8, NTHR = NWAVE * 64;
constexpr int KEYS_WG = SK / 2;
constexpr int ITEM_KEYS = 16;
constexpr int NITEM = KEYS_WG / (ITEM_KEYS * NWAVE);
constexpr unsigned SLICE_BYTES = KEYS_WG * DH * 2;
constexpr int NUNIT = FF / 8;

constexpr size_t WS_E = 0;
constexpr size_t WS_ML = 4096;
constexpr size_t WS_YA = 65536;
constexpr size_t WS_YG = WS_YA + 4 * 64 * 64 * 64;
constexpr size_t WS_END = WS_YG + 4 * 64 * 64 * 64;

constexpr int AUX_NT = 2;

__device__ __forceinline__ float dot2(unsigned a, unsigned b, float c) {
  return __builtin_amdgcn_fdot2_f32_bf16(__builtin_bit_cast(bf16x2, a), __builtin_bit_cast(bf16x2, b), c, false);
}
template <int CTRL> __device__ __forceinline__ float dpp(float x) {
  return __builtin_bit_cast(float, __builtin_amdgcn_mov_dpp(__builtin_bit_cast(int, x), CTRL, 0xf, 0xf, true));
}
constexpr int ROR8 = 0x128, ROR4 = 0x124, ROR2 = 0x122, ROR1 = 0x121;
__device__ __forceinline__ float row_allsum(float x) {
  x += dpp<ROR8>(x); x += dpp<ROR4>(x); x += dpp<ROR2>(x); x += dpp<ROR1>(x); return x;
}
__device__ __forceinline__ float swap32_sum(float a, float b) {
  auto s = __builtin_amdgcn_permlane32_swap(__float_as_uint(a), __float_as_uint(b), false, false);
  return __uint_as_float(s[0]) + __uint_as_float(s[1]);
}
__device__ __forceinline__ float swap16_sum(float a, float b) {
  auto s = __builtin_amdgcn_permlane16_swap(__float_as_uint(a), __float_as_uint(b), false, false);
  return __uint_as_float(s[0]) + __uint_as_float(s[1]);
}
__device__ __forceinline__ float xrow_max(float x) {
  auto s = __builtin_amdgcn_permlane16_swap(__float_as_uint(x), __float_as_uint(x), false, false);
  x = fmaxf(__uint_as_float(s[0]), __uint_as_float(s[1]));
  auto t = __builtin_amdgcn_permlane32_swap(__float_as_uint(x), __float_as_uint(x), false, false);
  return fmaxf(__uint_as_float(t[0]), __uint_as_float(t[1]));
}
__device__ __forceinline__ float xrow_sum(float x) { x = swap16_sum(x, x); return swap32_sum(x, x); }
__device__ __forceinline__ f32x2 xrow_treduce8(const float (&a)[8]) {
  float t0 = swap32_sum(a[0], a[4]), t1 = swap32_sum(a[1], a[5]), t2 = swap32_sum(a[2], a[6]), t3 = swap32_sum(a[3], a[7]);
  f32x2 r; r.x = swap16_sum(t0, t2); r.y = swap16_sum(t1, t3); return r;
}
__device__ __forceinline__ float bf_lo(unsigned w) { return __uint_as_float(w << 16); }
__device__ __forceinline__ float bf_hi(unsigned w) { return __uint_as_float(w & 0xffff0000u); }
__device__ __forceinline__ unsigned pack_bf16(float lo, float hi) {
  bf16x2 v; v.x = (__bf16)lo; v.y = (__bf16)hi; return __builtin_bit_cast(unsigned, v);
}
__device__ __forceinline__ float sigmoidf_(float z) { return 1.f / (1.f + __expf(-z)); }
__device__ __forceinline__ void store_granule(unsigned char* ws, size_t off, unsigned tag, float v) {
  __hip_atomic_store((gu64*)(ws + off), ((unsigned long long)tag << 32) | __float_as_uint(v), RLX_AGENT);
}
__device__ __forceinline__ unsigned long long load_granule(const unsigned char* ws, size_t off) {
  return __hip_atomic_load((gu64*)(ws + off), RLX_AGENT);
}
__device__ __forceinline__ void axpy8(float (&acc)[8], const u32x4& w, unsigned xl, unsigned xh) {
#pragma unroll
  for (int k = 0; k < 4; ++k) { acc[2 * k] = dot2(w[k], xl, acc[2 * k]); acc[2 * k + 1] = dot2(w[k], xh, acc[2 * k + 1]); }
}

struct Params {
  const unsigned short* x; const unsigned short* k; const unsigned short* v; const unsigned short* wq; const unsigned short* wo;
  const unsigned short* wg; const unsigned short* wu; const unsigned short* wd; const unsigned short* gw;
  unsigned short* out; unsigned char* ws;
};

constexpr int L_QP = 0;
constexpr int L_GU = L_QP + NWAVE * 128;
constexpr int L_ML = L_GU + 64;
constexpr int L_TAG = L_ML + 16;
constexpr int L_Y = L_TAG + 16;
constexpr int L_END = L_Y + NWAVE * 512;

__global__ void __launch_bounds__(NTHR, 2) fused_decode_kernel(Params p) {
  __shared__ __attribute__((aligned(16))) float lds[L_END];
  const int tid = threadIdx.x, lane = tid & 63, wave = __builtin_amdgcn_readfirstlane(tid >> 6);
  const int r = lane >> 4, c = lane & 15;
  const int bid = blockIdx.x, grp = bid & 7, j = bid >> 3;
  const int h = grp * 4 + (j >> 3), b = (j >> 1) & 3, s = j & 1;
  const int pidx = 2 * h + s;

  unsigned etag = 0;
  if (wave == 7) etag = __hip_atomic_load((gu32*)(p.ws + WS_E + b * 256), RLX_AGENT) + 1u;

  const __amdgpu_buffer_rsrc_t rq = __builtin_amdgcn_make_buffer_rsrc((void*)p.wq, 0, DM * NH * DH * 2, 0x00020000);
  const u32x4* xq_p = (const u32x4*)(p.x + b * DM + 64 * wave + 16 * r);
  const u32x4 xq0 = xq_p[0], xq1 = xq_p[1];
  u32x4 wreg[16];
  {
    const unsigned vo = ((64 * wave + 16 * r) * (NH * DH) + h * DH + 8 * c) * 2;
#pragma unroll
    for (int i = 0; i < 16; ++i) wreg[i] = __builtin_amdgcn_raw_buffer_load_b128(rq, vo, i * (NH * DH * 2), 0);
  }
  const size_t slice = ((size_t)(b * NH + h) * SK + (size_t)s * KEYS_WG) * DH;
  const __amdgpu_buffer_rsrc_t rk = __builtin_amdgcn_make_buffer_rsrc((void*)(p.k + slice), 0, SLICE_BYTES, 0x00020000);
  const __amdgpu_buffer_rsrc_t rv = __builtin_amdgcn_make_buffer_rsrc((void*)(p.v + slice), 0, SLICE_BYTES, 0x00020000);
  const unsigned lo16 = lane * 16;
  u32x4 kr[4], vr[4];
  unsigned soff = wave * 4096;
#pragma unroll
  for (int u = 0; u < 4; ++u) kr[u] = __builtin_amdgcn_raw_buffer_load_b128(rk, lo16 + u * 1024, soff, AUX_NT);
#pragma unroll
  for (int u = 0; u < 4; ++u) vr[u] = __builtin_amdgcn_raw_buffer_load_b128(rv, lo16 + u * 1024, soff, AUX_NT);

  const int nu = pidx < 44 ? 3 : 2, u0 = pidx < 44 ? 3 * pidx : 132 + 2 * (pidx - 44);
  const u32x4 xg = *(const u32x4*)(p.x + b * DM + 8 * lane);
  u32x4 greg[8];
  const int gm = wave >= 3 ? 1 : 0, gj = wave - 3 * gm;
  if (wave < 6) {
    const int un = min(u0 + gj, NUNIT - 1);
    const unsigned short* gp = (gm ? p.wu : p.wg) + (size_t)(8 * lane) * FF + 8 * un;
#pragma unroll
    for (int i = 0; i < 8; ++i) greg[i] = *(const u32x4*)(gp + i * FF);
  } else {
#pragma unroll
    for (int i = 0; i < 8; ++i) greg[i] = u32x4{0u, 0u, 0u, 0u};
    if (wave == 7) greg[0] = *(const u32x4*)(p.gw + 8 * lane);
  }
  unsigned short dreg[24];
#pragma unroll
  for (int i = 0; i < 24; ++i) dreg[i] = p.wd[(size_t)(min(8 * u0 + i, FF - 1)) * DM + tid];

  {
    float acc[8];
#pragma unroll
    for (int k = 0; k < 8; ++k) acc[k] = 0.f;
#pragma unroll
    for (int i = 0; i < 16; ++i) {
      const unsigned d = i < 8 ? xq0[(i >> 1) & 3] : xq1[(i >> 1) & 3];
      const unsigned xl = (i & 1) ? (d >> 16) : (d & 0xffffu), xh = (i & 1) ? (d & 0xffff0000u) : (d << 16);
      axpy8(acc, wreg[i], xl, xh);
    }
    const f32x2 t = xrow_treduce8(acc);
    *(f32x2*)(lds + L_QP + wave * 128 + 8 * c + 2 * r) = t;
  }
  float gate = 0.f;
  if (wave < 6) {
    float acc[8];
#pragma unroll
    for (int k = 0; k < 8; ++k) acc[k] = 0.f;
#pragma unroll
    for (int i = 0; i < 8; ++i) {
      const unsigned d = xg[i >> 1];
      const unsigned xl = (i & 1) ? (d >> 16) : (d & 0xffffu), xh = (i & 1) ? (d & 0xffff0000u) : (d << 16);
      axpy8(acc, greg[i], xl, xh);
    }
    f32x2 t = xrow_treduce8(acc);
    t.x = row_allsum(t.x); t.y = row_allsum(t.y);
    if (c == 0) *(f32x2*)(lds + L_GU + gm * 24 + gj * 8 + 2 * r) = t;
  } else if (wave == 7) {
    float z = 0.f;
#pragma unroll
    for (int k = 0; k < 4; ++k) z = dot2(xg[k], greg[0][k], z);
    z = xrow_sum(row_allsum(z));
    gate = sigmoidf_(z);
    if (lane == 0) ((unsigned*)lds)[L_TAG] = etag;
  }
  __syncthreads();

  u32x4 qp;
  {
    float a[8];
    const float* q0 = lds + L_QP + (2 * r) * 128 + 8 * c;
    const f32x4 x0 = *(const f32x4*)(q0), x1 = *(const f32x4*)(q0 + 4), y0 = *(const f32x4*)(q0 + 128), y1 = *(const f32x4*)(q0 + 132);
#pragma unroll
    for (int k = 0; k < 4; ++k) { a[k] = xrow_sum(x0[k] + y0[k]); a[k + 4] = xrow_sum(x1[k] + y1[k]); }
#pragma unroll
    for (int k = 0; k < 4; ++k) qp[k] = pack_bf16(a[2 * k], a[2 * k + 1]);
  }
  const unsigned tag = __builtin_amdgcn_readfirstlane(((const unsigned*)lds)[L_TAG]);
  {
    const u32x4* wop = (const u32x4*)(p.wo + (size_t)(h * DH + 16 * wave) * DM + 8 * lane);
#pragma unroll
    for (int i = 0; i < 16; ++i) wreg[i] = wop[i * (DM / 8)];
  }
  {
    const int f = min(lane, 23);
    const float gv = lds[L_GU + f], uv = lds[L_GU + 24 + f];
    const float hv = (lane < 8 * nu) ? sigmoidf_(gv) * uv : 0.f;
    float y = 0.f;
#pragma unroll
    for (int i = 0; i < 24; ++i)
      y = fmaf(__builtin_bit_cast(float, __builtin_amdgcn_readlane(__builtin_bit_cast(int, hv), i)), __uint_as_float((unsigned)dreg[i] << 16), y);
    store_granule(p.ws, WS_YG + ((size_t)((b * 64 + (tid >> 3)) * 64 + pidx) * 8 + (tid & 7)) * 8, tag, y);
  }

  constexpr float C2 = 0.08838834764831845f * 1.4426950408889634f;
  float m = -INFINITY, l = 0.f, acc[8];
#pragma unroll
  for (int k = 0; k < 8; ++k) acc[k] = 0.f;
#pragma unroll 1
  for (int it = 0; it < NITEM; ++it) {
    soff += NWAVE * 4096;
    float sc[4];
#pragma unroll
    for (int u = 0; u < 4; ++u) {
      float d = dot2(kr[u][0], qp[0], 0.f);
      d = dot2(kr[u][1], qp[1], d); d = dot2(kr[u][2], qp[2], d); d = dot2(kr[u][3], qp[3], d);
      kr[u] = __builtin_amdgcn_raw_buffer_load_b128(rk, lo16 + u * 1024, soff, AUX_NT);
      sc[u] = row_allsum(d) * C2;
    }
    const float mn = fmaxf(fmaxf(fmaxf(sc[0], sc[1]), fmaxf(sc[2], sc[3])), m);
    const float alpha = __builtin_amdgcn_exp2f(m - mn);
    m = mn;
    float pr[4];
#pragma unroll
    for (int u = 0; u < 4; ++u) pr[u] = __builtin_amdgcn_exp2f(sc[u] - mn);
    l = fmaf(l, alpha, (pr[0] + pr[1]) + (pr[2] + pr[3]));
#pragma unroll
    for (int k = 0; k < 8; ++k) acc[k] *= alpha;
#pragma unroll
    for (int u = 0; u < 4; ++u) {
#pragma unroll
      for (int k = 0; k < 4; ++k) {
        acc[2 * k] = fmaf(pr[u], bf_lo(vr[u][k]), acc[2 * k]);
        acc[2 * k + 1] = fmaf(pr[u], bf_hi(vr[u][k]), acc[2 * k + 1]);
      }
      vr[u] = __builtin_amdgcn_raw_buffer_load_b128(rv, lo16 + u * 1024, soff, AUX_NT);
    }
  }

  {
    const float mw = xrow_max(m);
    const float sw = __builtin_amdgcn_exp2f(m - mw);
#pragma unroll
    for (int k = 0; k < 8; ++k) acc[k] *= sw;
    const float lw = xrow_sum(l * sw);
    const f32x2 t = xrow_treduce8(acc);
    *(f32x2*)(lds + L_QP + wave * 128 + 8 * c + 2 * r) = t;
    if (lane == 0) *(f32x2*)(lds + L_ML + 2 * wave) = f32x2{mw, lw};
  }
  __syncthreads();
  {
    float mv[8], lv[8];
#pragma unroll
    for (int w = 0; w < 8; w += 2) { const f32x4 t = *(const f32x4*)(lds + L_ML + 2 * w); mv[w] = t[0]; lv[w] = t[1]; mv[w + 1] = t[2]; lv[w + 1] = t[3]; }
    float M = mv[0];
#pragma unroll
    for (int w = 1; w < 8; ++w) M = fmaxf(M, mv[w]);
    const f32x4 mine = *(const f32x4*)(lds + L_ML + 4 * r);
    const float s0 = __builtin_amdgcn_exp2f(mine[0] - M), s1 = __builtin_amdgcn_exp2f(mine[2] - M);
    float o = s0 * lds[L_QP + (2 * r) * 128 + 16 * wave + c] + s1 * lds[L_QP + (2 * r + 1) * 128 + 16 * wave + c];
    o = xrow_sum(o);
    float y[8];
#pragma unroll
    for (int k = 0; k < 8; ++k) y[k] = 0.f;
#pragma unroll
    for (int i = 0; i < 16; ++i) {
      const float oi = __builtin_bit_cast(float, __builtin_amdgcn_readlane(__builtin_bit_cast(int, o), i));
#pragma unroll
      for (int k = 0; k < 4; ++k) { y[2 * k] = fmaf(oi, bf_lo(wreg[i][k]), y[2 * k]); y[2 * k + 1] = fmaf(oi, bf_hi(wreg[i][k]), y[2 * k + 1]); }
    }
    *(f32x4*)(lds + L_Y + wave * 512 + 8 * lane) = f32x4{y[0], y[1], y[2], y[3]};
    *(f32x4*)(lds + L_Y + wave * 512 + 8 * lane + 4) = f32x4{y[4], y[5], y[6], y[7]};
    if (wave == 6) {
      float L = 0.f;
#pragma unroll
      for (int w = 0; w < 8; ++w) L = fmaf(__builtin_amdgcn_exp2f(mv[w] - M), lv[w], L);
      if (lane < 2) store_granule(p.ws, WS_ML + ((size_t)(b * 2 + lane) * 64 + pidx) * 8, tag, lane ? L : M);
    }
  }
  __syncthreads();
  {
    float y = 0.f;
#pragma unroll
    for (int w = 0; w < 8; ++w) y += lds[L_Y + w * 512 + tid];
    store_granule(p.ws, WS_YA + ((size_t)((b * 64 + (tid >> 3)) * 64 + pidx) * 8 + (tid & 7)) * 8, tag, y);
  }
  if (wave != 7) return;

  {
    const size_t gbase = (size_t)(b * 64 + pidx) * 64 * 64 + lane * 8;
    float ya[8], yg[8], mm = 0.f, ll = 0.f;
    unsigned pend = 0x3ffffu;
    for (unsigned spins = 0; pend != 0u && spins < (1u << 22); ++spins) {
      unsigned long long ga[8], gg[8], gm_ = 0, gl_ = 0;
#pragma unroll
      for (int jj = 0; jj < 8; ++jj) { ga[jj] = 0; if (pend & (1u << jj)) ga[jj] = load_granule(p.ws, WS_YA + gbase + jj * 512); }
#pragma unroll
      for (int jj = 0; jj < 8; ++jj) { gg[jj] = 0; if (pend & (256u << jj)) gg[jj] = load_granule(p.ws, WS_YG + gbase + jj * 512); }
      if (pend & (1u << 16)) gm_ = load_granule(p.ws, WS_ML + ((size_t)(b * 2) * 64 + lane) * 8);
      if (pend & (1u << 17)) gl_ = load_granule(p.ws, WS_ML + ((size_t)(b * 2 + 1) * 64 + lane) * 8);
#pragma unroll
      for (int jj = 0; jj < 8; ++jj) {
        if ((pend & (1u << jj)) && __all((unsigned)(ga[jj] >> 32) == tag)) { ya[jj] = __uint_as_float((unsigned)ga[jj]); pend &= ~(1u << jj); }
        if ((pend & (256u << jj)) && __all((unsigned)(gg[jj] >> 32) == tag)) { yg[jj] = __uint_as_float((unsigned)gg[jj]); pend &= ~(256u << jj); }
      }
      if ((pend & (1u << 16)) && __all((unsigned)(gm_ >> 32) == tag)) { mm = __uint_as_float((unsigned)gm_); pend &= ~(1u << 16); }
      if ((pend & (1u << 17)) && __all((unsigned)(gl_ >> 32) == tag)) { ll = __uint_as_float((unsigned)gl_); pend &= ~(1u << 17); }
      if (pend) __builtin_amdgcn_s_sleep(1);
    }
    const float mo = dpp<0xB1>(mm), lo_ = dpp<0xB1>(ll);
    const float M = fmaxf(mm, mo);
    const float w0 = __builtin_amdgcn_exp2f(mm - M), w1 = __builtin_amdgcn_exp2f(mo - M);
    const float scale = gate * w0 / fmaf(w0, ll, w1 * lo_);
    float accA = 0.f, accG = 0.f;
#pragma unroll
    for (int jj = 0; jj < 8; ++jj) {
      const float sj = __shfl(scale, 8 * jj + (lane >> 3));
      accA = fmaf(sj, ya[jj], accA); accG += yg[jj];
    }
    float o = fmaf(1.f - gate, accG, accA);
    o += dpp<ROR8>(o);
    o = xrow_sum(o);
    if (pend) o = __builtin_nanf("");
    const unsigned short ob = __builtin_bit_cast(unsigned short, (__bf16)o);
    if (lane < 8) p.out[b * DM + 8 * pidx + lane] = ob;
    if (pidx == 0 && lane == 0) __hip_atomic_store((gu32*)(p.ws + WS_E + b * 256), tag, RLX_AGENT);
  }
}

}

extern "C" void kernel_launch(void* const* d_in, const int* in_sizes, int n_in, void* d_out, int out_size, void* d_ws,
                              size_t ws_size, hipStream_t stream) {
  (void)in_sizes; (void)n_in; (void)out_size; (void)ws_size;
  Params p;
  p.x = (const unsigned short*)d_in[0]; p.k = (const unsigned short*)d_in[1]; p.v = (const unsigned short*)d_in[2];
  p.wq = (const unsigned short*)d_in[3]; p.wo = (const unsigned short*)d_in[4];
  p.wg = (const unsigned short*)d_in[6]; p.wu = (const unsigned short*)d_in[7]; p.wd = (const unsigned short*)d_in[8];
  p.gw = (const unsigned short*)d_in[9];
  p.out = (unsigned short*)d_out; p.ws = (unsigned char*)d_ws;
  hipLaunchKernelGGL(fused_decode_kernel, dim3(NWG), dim3(NTHR), 0, stream, p);
}
```

```cpp
#include <hip/hip_runtime.h>
#include <hip/hip_bf16.h>
#include <stdint.h>

typedef unsigned u32x4 __attribute__((ext_vector_type(4)));
typedef unsigned u32x2 __attribute__((ext_vector_type(2)));
typedef float f32x2 __attribute__((ext_vector_type(2)));
typedef float f32x4 __attribute__((ext_vector_type(4)));
typedef __bf16 bf16x2 __attribute__((ext_vector_type(2)));
#define GAS __attribute__((address_space(1)))
#define LAS __attribute__((address_space(3)))
typedef GAS unsigned gu32;
typedef GAS unsigned long long gu64;
#define RLX_AGENT __ATOMIC_RELAXED, __HIP_MEMORY_SCOPE_AGENT

namespace {

constexpr int DM = 512, NH = 32, SK = 4096, DH = 128, FF = 1376;
constexpr int NWG = 256, NSW = 8, NWAVE = NSW + 1, NTHR = NWAVE * 64;
constexpr int KEYS_WG = SK / 2;
constexpr int ITEM_KEYS = 16;
constexpr int NITEM = KEYS_WG / (ITEM_KEYS * NSW);
constexpr unsigned SLICE_BYTES = KEYS_WG * DH * 2;
constexpr int NGLU = FF / 32;

constexpr size_t WS_E = 0;
constexpr size_t WS_GC = 2048;
constexpr size_t WS_ML = 4096;
constexpr size_t WS_YA = 65536;
constexpr size_t WS_YG = WS_YA + 4 * 64 * 64 * 64;
constexpr size_t WS_G1 = WS_YG + 4 * 64 * 64 * 64;
constexpr size_t WS_END = WS_G1 + 43 * 4 * 4 * 64 * 8;

constexpr int AUX_NT = 2;
#define WQ_BARRIER 1
#define MODE_B 0
#define X_GU(x) (x)
#define X_WD(x) (x)
#define X_ST 1
#define G1_AT 9
#define GLU_AT 14
#define NPRE_ 1
constexpr int NPRE = NPRE_;
#define STAMP(k) do {} while (0)
#define STAMP0(k) do {} while (0)

__device__ __forceinline__ float dot2(unsigned a, unsigned b, float c) {
  return __builtin_amdgcn_fdot2_f32_bf16(__builtin_bit_cast(bf16x2, a), __builtin_bit_cast(bf16x2, b), c, false);
}
template <int CTRL> __device__ __forceinline__ float dpp(float x) {
  return __builtin_bit_cast(float, __builtin_amdgcn_mov_dpp(__builtin_bit_cast(int, x), CTRL, 0xf, 0xf, true));
}
constexpr int ROR8 = 0x128, ROR4 = 0x124, ROR2 = 0x122, ROR1 = 0x121;
__device__ __forceinline__ float row_allsum(float x) {
  x += dpp<ROR8>(x); x += dpp<ROR4>(x); x += dpp<ROR2>(x); x += dpp<ROR1>(x); return x;
}
__device__ __forceinline__ float swap32_sum(float a, float b) {
  auto s = __builtin_amdgcn_permlane32_swap(__float_as_uint(a), __float_as_uint(b), false, false);
  return __uint_as_float(s[0]) + __uint_as_float(s[1]);
}
__device__ __forceinline__ float swap16_sum(float a, float b) {
  auto s = __builtin_amdgcn_permlane16_swap(__float_as_uint(a), __float_as_uint(b), false, false);
  return __uint_as_float(s[0]) + __uint_as_float(s[1]);
}
__device__ __forceinline__ float xrow_max(float x) {
  auto s = __builtin_amdgcn_permlane16_swap(__float_as_uint(x), __float_as_uint(x), false, false);
  x = fmaxf(__uint_as_float(s[0]), __uint_as_float(s[1]));
  auto t = __builtin_amdgcn_permlane32_swap(__float_as_uint(x), __float_as_uint(x), false, false);
  return fmaxf(__uint_as_float(t[0]), __uint_as_float(t[1]));
}
__device__ __forceinline__ float xrow_sum(float x) { x = swap16_sum(x, x); return swap32_sum(x, x); }
__device__ __forceinline__ f32x2 xrow_treduce8(const float (&a)[8]) {
  float t0 = swap32_sum(a[0], a[4]), t1 = swap32_sum(a[1], a[5]), t2 = swap32_sum(a[2], a[6]), t3 = swap32_sum(a[3], a[7]);
  f32x2 r; r.x = swap16_sum(t0, t2); r.y = swap16_sum(t1, t3); return r;
}
__device__ __forceinline__ float bf_lo(unsigned w) { return __uint_as_float(w << 16); }
__device__ __forceinline__ float bf_hi(unsigned w) { return __uint_as_float(w & 0xffff0000u); }
__device__ __forceinline__ unsigned pack_bf16(float lo, float hi) {
  bf16x2 v; v.x = (__bf16)lo; v.y = (__bf16)hi; return __builtin_bit_cast(unsigned, v);
}
__device__ __forceinline__ float sigmoidf_(float z) { return 1.f / (1.f + __expf(-z)); }
__device__ __forceinline__ void store_granule(unsigned char* ws, size_t off, unsigned tag, float v) {
  __hip_atomic_store((gu64*)(ws + off), ((unsigned long long)tag << 32) | __float_as_uint(v), RLX_AGENT);
}
__device__ __forceinline__ unsigned long long load_granule(const unsigned char* ws, size_t off) {
  return __hip_atomic_load((gu64*)(ws + off), RLX_AGENT);
}
__device__ __forceinline__ void axpy8(float (&acc)[8], const u32x4& w, unsigned xl, unsigned xh) {
#pragma unroll
  for (int k = 0; k < 4; ++k) { acc[2 * k] = dot2(w[k], xl, acc[2 * k]); acc[2 * k + 1] = dot2(w[k], xh, acc[2 * k + 1]); }
}

struct Params {
  const unsigned short* x; const unsigned short* k; const unsigned short* v; const unsigned short* wq; const unsigned short* wo;
  const unsigned short* wg; const unsigned short* wu; const unsigned short* wd; const unsigned short* gw;
  unsigned short* out; unsigned char* ws;
};

constexpr int L_WO = 0;
constexpr int L_QP = L_WO + 128 * 256;
constexpr int L_ML = L_QP + NSW * 128;
constexpr int L_TAG = L_ML + 16;
constexpr int L_Y = L_TAG + 16;
constexpr int L_END = L_Y + NSW * 512;

constexpr float C2 = 0.08838834764831845f * 1.4426950408889634f;

#define PIN8(a) asm volatile("" : "+v"(a[0]), "+v"(a[1]), "+v"(a[2]), "+v"(a[3]), "+v"(a[4]), "+v"(a[5]), "+v"(a[6]), "+v"(a[7]))
#define SB() __builtin_amdgcn_sched_barrier(0)

__device__ __forceinline__ float qk_dot(const u32x4& k, const u32x4& q) {
  float d = dot2(k[0], q[0], 0.f);
  d = dot2(k[1], q[1], d); d = dot2(k[2], q[2], d); return dot2(k[3], q[3], d);
}
__device__ __forceinline__ void softmax_step(const float (&sc)[4], float& m, float& l, float (&acc)[8], float (&pr)[4]) {
  const float mn = fmaxf(fmaxf(fmaxf(sc[0], sc[1]), fmaxf(sc[2], sc[3])), m);
  const float alpha = __builtin_amdgcn_exp2f(m - mn);
  m = mn;
#pragma unroll
  for (int u = 0; u < 4; ++u) pr[u] = __builtin_amdgcn_exp2f(sc[u] - mn);
  l = fmaf(l, alpha, (pr[0] + pr[1]) + (pr[2] + pr[3]));
#pragma unroll
  for (int k = 0; k < 8; ++k) acc[k] *= alpha;
}
__device__ __forceinline__ void pv_step(float (&acc)[8], float p, const u32x4& v) {
#pragma unroll
  for (int k = 0; k < 4; ++k) { acc[2 * k] = fmaf(p, bf_lo(v[k]), acc[2 * k]); acc[2 * k + 1] = fmaf(p, bf_hi(v[k]), acc[2 * k + 1]); }
}

__global__ void __launch_bounds__(NTHR, 3) fused_decode_kernel(Params p) {
  __shared__ __attribute__((aligned(16))) float lds[L_END];
  const int tid = threadIdx.x, lane = tid & 63, wave = __builtin_amdgcn_readfirstlane(tid >> 6);
  const int r = lane >> 4, c = lane & 15;
  const int bid = blockIdx.x, grp = bid & 7, j = bid >> 3;
  const int h = grp * 4 + (j >> 3), b = (j >> 1) & 3, s = j & 1;
  const int pidx = 2 * h + s;
  const unsigned lo16 = lane * 16;
  STAMP(0);

  if (wave < NSW) {
    const __amdgpu_buffer_rsrc_t rq = __builtin_amdgcn_make_buffer_rsrc((void*)p.wq, 0, DM * NH * DH * 2, 0x00020000);
    const u32x4* xq_p = (const u32x4*)(p.x + b * DM + 64 * wave + 16 * r);
    const u32x4 xq0 = xq_p[0], xq1 = xq_p[1];
    u32x4 wreg[16];
    {
      const unsigned vo = ((64 * wave + 16 * r) * (NH * DH) + h * DH + 8 * c) * 2;
#pragma unroll
      for (int i = 0; i < 16; ++i) wreg[i] = __builtin_amdgcn_raw_buffer_load_b128(rq, vo, i * (NH * DH * 2), AUX_NT);
    }
    SB();
    const size_t slice = ((size_t)(b * NH + h) * SK + (size_t)s * KEYS_WG) * DH;
    const __amdgpu_buffer_rsrc_t rk = __builtin_amdgcn_make_buffer_rsrc((void*)(p.k + slice), 0, SLICE_BYTES, 0x00020000);
    const __amdgpu_buffer_rsrc_t rv = __builtin_amdgcn_make_buffer_rsrc((void*)(p.v + slice), 0, SLICE_BYTES, 0x00020000);
    const __amdgpu_buffer_rsrc_t ro = __builtin_amdgcn_make_buffer_rsrc((void*)(p.wo + (size_t)(h * DH + 16 * wave) * DM), 0, 16 * DM * 2, 0x00020000);
    __builtin_amdgcn_s_barrier();
    u32x4 pk[NPRE][4], pv[NPRE][4];
    unsigned soff = wave * 4096;
#pragma unroll
    for (int n = 0; n < NPRE; ++n) {
#pragma unroll
      for (int u = 0; u < 4; ++u) pk[n][u] = __builtin_amdgcn_raw_buffer_load_b128(rk, lo16 + u * 1024, soff + n * (NSW * 4096), AUX_NT);
#pragma unroll
      for (int u = 0; u < 4; ++u) pv[n][u] = __builtin_amdgcn_raw_buffer_load_b128(rv, lo16 + u * 1024, soff + n * (NSW * 4096), AUX_NT);
    }
    SB();
    {
      float acc[8];
#pragma unroll
      for (int k = 0; k < 8; ++k) acc[k] = 0.f;
#pragma unroll
      for (int i = 0; i < 16; ++i) {
        const unsigned d = i < 8 ? xq0[(i >> 1) & 3] : xq1[(i >> 1) & 3];
        const unsigned xl = (i & 1) ? (d >> 16) : (d & 0xffffu), xh = (i & 1) ? (d & 0xffff0000u) : (d << 16);
        axpy8(acc, wreg[i], xl, xh);
      }
      const f32x2 t = xrow_treduce8(acc);
      *(f32x2*)(lds + L_QP + wave * 128 + 8 * c + 2 * r) = t;
    }
    SB();
    soff += NPRE * (NSW * 4096);
    u32x4 a[4];
#pragma unroll
    for (int u = 0; u < 4; ++u) a[u] = __builtin_amdgcn_raw_buffer_load_b128(rk, lo16 + u * 1024, soff, AUX_NT);
    u32x4 wo_r = __builtin_amdgcn_raw_buffer_load_b128(ro, lo16, 0, AUX_NT);
    SB();
    __syncthreads();
    STAMP(1);
    u32x4 qp;
    {
      float qa[8];
      const float* q0 = lds + L_QP + (2 * r) * 128 + 8 * c;
      const f32x4 x0 = *(const f32x4*)(q0), x1 = *(const f32x4*)(q0 + 4), y0 = *(const f32x4*)(q0 + 128), y1 = *(const f32x4*)(q0 + 132);
#pragma unroll
      for (int k = 0; k < 4; ++k) { qa[k] = xrow_sum(x0[k] + y0[k]); qa[k + 4] = xrow_sum(x1[k] + y1[k]); }
#pragma unroll
      for (int k = 0; k < 4; ++k) qp[k] = pack_bf16(qa[2 * k], qa[2 * k + 1]);
    }
    float m = -INFINITY, l = 0.f, acc[8];
#pragma unroll
    for (int k = 0; k < 8; ++k) acc[k] = 0.f;
#pragma unroll
    for (int n = 0; n < NPRE; ++n) {
      float sc[4], pr[4];
#pragma unroll
      for (int u = 0; u < 4; ++u) sc[u] = row_allsum(qk_dot(pk[n][u], qp)) * C2;
      softmax_step(sc, m, l, acc, pr);
#pragma unroll
      for (int u = 0; u < 4; ++u) pv_step(acc, pr[u], pv[n][u]);
      PIN8(acc); SB();
    }
    u32x4* wo_lds = (u32x4*)(lds + L_WO) + wave * 1024 + lane;
#pragma unroll 1
    for (int it = NPRE; it < NITEM; ++it) {
      wo_lds[(it - 1) * 64] = wo_r;
      if (wave == 0) __hip_atomic_store((LAS unsigned*)lds + L_TAG + 1, (unsigned)it, __ATOMIC_RELAXED, __HIP_MEMORY_SCOPE_WORKGROUP);
      SB();
      float sc[4], pr[4];
#pragma unroll
      for (int u = 0; u < 4; ++u) {
        const float d = qk_dot(a[u], qp);
        SB();
        a[u] = __builtin_amdgcn_raw_buffer_load_b128(rv, lo16 + u * 1024, soff, AUX_NT);
        SB();
        sc[u] = row_allsum(d) * C2;
      }
      wo_r = __builtin_amdgcn_raw_buffer_load_b128(ro, lo16, it * 1024, AUX_NT);
      softmax_step(sc, m, l, acc, pr);
      soff += NSW * 4096;
      SB();
#pragma unroll
      for (int u = 0; u < 4; ++u) {
        pv_step(acc, pr[u], a[u]);
        PIN8(acc); SB();
        a[u] = __builtin_amdgcn_raw_buffer_load_b128(rk, lo16 + u * 1024, soff, AUX_NT);
        SB();
      }
    }
    STAMP(3);
#pragma unroll
    for (int i = 0; i < 15; ++i) wreg[i] = wo_lds[i * 64];
    wreg[15] = wo_r;
    {
      const float mw = xrow_max(m);
      const float sw = __builtin_amdgcn_exp2f(m - mw);
#pragma unroll
      for (int k = 0; k < 8; ++k) acc[k] *= sw;
      const float lw = xrow_sum(l * sw);
      const f32x2 t = xrow_treduce8(acc);
      *(f32x2*)(lds + L_QP + wave * 128 + 8 * c + 2 * r) = t;
      if (lane == 0) *(f32x2*)(lds + L_ML + 2 * wave) = f32x2{mw, lw};
    }
    __syncthreads();
    const unsigned tag = __builtin_amdgcn_readfirstlane(((const unsigned*)lds)[L_TAG]);
    const int gi = __builtin_amdgcn_readfirstlane(((const int*)lds)[L_TAG + 2]);
    u32x4 wdr[4];
    unsigned long long gg = 0, gu = 0;
    const size_t g1off = WS_G1 + ((size_t)((gi * 4 + r) * 4 + (c & 3)) * 64 + 4 * wave + (c >> 2)) * 8;
    STAMP(4);
    if (gi < NGLU) {
      const __amdgpu_buffer_rsrc_t rd = __builtin_amdgcn_make_buffer_rsrc((void*)(p.wd + (size_t)(gi * 32 + 4 * wave) * DM), 0, 4 * DM * 2, 0x00020000);
      gg = load_granule(p.ws, g1off); gu = load_granule(p.ws, g1off + 256);
#pragma unroll
      for (int i = 0; i < 4; ++i) wdr[i] = __builtin_amdgcn_raw_buffer_load_b128(rd, lo16, i * 1024, AUX_NT);
    }
    SB();
    {
      float mv[8], lv[8];
#pragma unroll
      for (int w = 0; w < 8; w += 2) { const f32x4 t = *(const f32x4*)(lds + L_ML + 2 * w); mv[w] = t[0]; lv[w] = t[1]; mv[w + 1] = t[2]; lv[w + 1] = t[3]; }
      float M = mv[0];
#pragma unroll
      for (int w = 1; w < 8; ++w) M = fmaxf(M, mv[w]);
      const f32x4 mine = *(const f32x4*)(lds + L_ML + 4 * r);
      const float s0 = __builtin_amdgcn_exp2f(mine[0] - M), s1 = __builtin_amdgcn_exp2f(mine[2] - M);
      float o = s0 * lds[L_QP + (2 * r) * 128 + 16 * wave + c] + s1 * lds[L_QP + (2 * r + 1) * 128 + 16 * wave + c];
      o = xrow_sum(o);
      float y[8];
#pragma unroll
      for (int k = 0; k < 8; ++k) y[k] = 0.f;
#pragma unroll
      for (int i = 0; i < 16; ++i) {
        const float oi = __builtin_bit_cast(float, __builtin_amdgcn_readlane(__builtin_bit_cast(int, o), i));
        pv_step(y, oi, wreg[i]);
        if (i & 1) { PIN8(y); SB(); }
      }
      *(f32x4*)(lds + L_Y + wave * 512 + 8 * lane) = f32x4{y[0], y[1], y[2], y[3]};
      *(f32x4*)(lds + L_Y + wave * 512 + 8 * lane + 4) = f32x4{y[4], y[5], y[6], y[7]};
      if (wave == 6) {
        float L = 0.f;
#pragma unroll
        for (int w = 0; w < 8; ++w) L = fmaf(__builtin_amdgcn_exp2f(mv[w] - M), lv[w], L);
        if (lane < 2) store_granule(p.ws, WS_ML + ((size_t)(b * 2 + lane) * 64 + pidx) * 8, tag, lane ? L : M);
      }
    }
    __syncthreads();
    STAMP(5);
    {
      float y = 0.f;
#pragma unroll
      for (int w = 0; w < 8; ++w) y += lds[L_Y + w * 512 + tid];
      store_granule(p.ws, WS_YA + ((size_t)((b * 64 + (tid >> 3)) * 64 + pidx) * 8 + (tid & 7)) * 8, tag, y);
    }
    STAMP(6);
    if (gi >= NGLU) return;
    float* gp = lds + L_WO;
    {
      const unsigned tag0 = ((const unsigned*)lds)[L_TAG + 4];
      for (unsigned spins = 0; spins < (1u << 22); ++spins) {
        if (__all((unsigned)(gg >> 32) == tag0 && (unsigned)(gu >> 32) == tag0)) break;
        __builtin_amdgcn_s_sleep(4);
        gg = load_granule(p.ws, g1off); gu = load_granule(p.ws, g1off + 256);
      }
      const float gs = xrow_sum(__uint_as_float((unsigned)gg)), us = xrow_sum(__uint_as_float((unsigned)gu));
      const float hv = sigmoidf_(gs) * us;
      float* yp = gp + 4096 + wave * 2048 + 8 * lane;
#pragma unroll
      for (int bb = 0; bb < 4; ++bb) {
        float y[8];
#pragma unroll
        for (int k = 0; k < 8; ++k) y[k] = 0.f;
#pragma unroll
        for (int i = 0; i < 4; ++i)
          pv_step(y, __builtin_bit_cast(float, __builtin_amdgcn_readlane(__builtin_bit_cast(int, hv), 4 * i + bb)), wdr[i]);
        *(f32x4*)(yp + bb * 512) = f32x4{y[0], y[1], y[2], y[3]};
        *(f32x4*)(yp + bb * 512 + 4) = f32x4{y[4], y[5], y[6], y[7]};
      }
    }
    __syncthreads();
#pragma unroll
    for (int bb = 0; bb < 4; ++bb) {
      float y = 0.f;
#pragma unroll
      for (int w = 0; w < 8; ++w) y += gp[4096 + w * 2048 + bb * 512 + tid];
      store_granule(p.ws, WS_YG + ((size_t)((bb * 64 + (tid >> 3)) * 64 + gi) * 8 + (tid & 7)) * 8, ((const unsigned*)lds)[L_TAG + 4 + bb], y);
    }
    STAMP(2);
    return;
  }

  const unsigned tags = __hip_atomic_load((gu32*)(p.ws + WS_E + (lane & 3) * 256), RLX_AGENT) + 1u;
  __builtin_amdgcn_s_barrier();
  SB();
  const unsigned tag = __builtin_amdgcn_readlane(tags, b);
  if (lane == 0) { ((unsigned*)lds)[L_TAG] = tag; ((unsigned*)lds)[L_TAG + 1] = 0u; }
  if (lane < 4) ((unsigned*)lds)[L_TAG + 4 + lane] = tags;
  if (bid == 0 && lane < 2) __hip_atomic_store((gu64*)(p.ws + WS_GC + lane * 256), (unsigned long long)tag << 32, RLX_AGENT);
  __syncthreads();
  float gate;
  {
    const u32x4 xg = *(const u32x4*)(p.x + b * DM + 8 * lane), gw = *(const u32x4*)(p.gw + 8 * lane);
    float z = 0.f;
#pragma unroll
    for (int k = 0; k < 4; ++k) z = dot2(xg[k], gw[k], z);
    gate = sigmoidf_(xrow_sum(row_allsum(z)));
  }
  const unsigned tag0 = __builtin_amdgcn_readlane(tags, 0);
  auto claim = [&](size_t off, unsigned at) -> unsigned {
    while (__hip_atomic_load((LAS unsigned*)lds + L_TAG + 1, __ATOMIC_RELAXED, __HIP_MEMORY_SCOPE_WORKGROUP) < at) __builtin_amdgcn_s_sleep(8);
#pragma unroll 1
    for (unsigned tries = 0; tries < 65536u; ++tries) {
      unsigned long long tk = 0;
      if (lane == 0) tk = __hip_atomic_fetch_add((gu64*)(p.ws + off), 1ull, RLX_AGENT);
      if (__builtin_amdgcn_readfirstlane((unsigned)(tk >> 32)) == tag0) return __builtin_amdgcn_readfirstlane((unsigned)tk);
      __builtin_amdgcn_s_sleep(32);
    }
    return 0xffffu;
  };
  const unsigned t1 = claim(WS_GC + 256, G1_AT);
  if (t1 < 4u * NGLU) {
    const int cg = t1 >> 2, rb = t1 & 3, row0 = 128 * rb + 16 * (lane >> 3);
    const __amdgpu_buffer_rsrc_t rg = __builtin_amdgcn_make_buffer_rsrc((void*)p.wg, 0, DM * FF * 2, 0x00020000);
    const __amdgpu_buffer_rsrc_t ru = __builtin_amdgcn_make_buffer_rsrc((void*)p.wu, 0, DM * FF * 2, 0x00020000);
    const unsigned vo = (row0 * FF + 32 * cg + 4 * (lane & 7)) * 2;
    u32x4 xr[4][2];
#pragma unroll
    for (int bb = 0; bb < 4; ++bb) { xr[bb][0] = *(const u32x4*)(p.x + bb * DM + row0); xr[bb][1] = *(const u32x4*)(p.x + bb * DM + row0 + 8); }
    u32x2 wgr[16], wur[16];
#pragma unroll
    for (int i = 0; i < 16; ++i) { wgr[i] = __builtin_amdgcn_raw_buffer_load_b64(rg, vo, i * (FF * 2), AUX_NT); wur[i] = __builtin_amdgcn_raw_buffer_load_b64(ru, vo, i * (FF * 2), AUX_NT); }
    SB();
#pragma unroll 1
    for (int bb = 0; bb < 4; ++bb) {
      const u32x4 x0 = bb == 0 ? xr[0][0] : bb == 1 ? xr[1][0] : bb == 2 ? xr[2][0] : xr[3][0];
      const u32x4 x1 = bb == 0 ? xr[0][1] : bb == 1 ? xr[1][1] : bb == 2 ? xr[2][1] : xr[3][1];
      float ag[4] = {0.f, 0.f, 0.f, 0.f}, au[4] = {0.f, 0.f, 0.f, 0.f};
#pragma unroll
      for (int i = 0; i < 16; ++i) {
        const unsigned d = i < 8 ? x0[(i >> 1) & 3] : x1[(i >> 1) & 3];
        const unsigned xl = (i & 1) ? (d >> 16) : (d & 0xffffu), xh = (i & 1) ? (d & 0xffff0000u) : (d << 16);
#pragma unroll
        for (int k = 0; k < 2; ++k) {
          ag[2 * k] = dot2(wgr[i][k], xl, ag[2 * k]); ag[2 * k + 1] = dot2(wgr[i][k], xh, ag[2 * k + 1]);
          au[2 * k] = dot2(wur[i][k], xl, au[2 * k]); au[2 * k + 1] = dot2(wur[i][k], xh, au[2 * k + 1]);
        }
      }
      float tg = swap16_sum(swap32_sum(ag[0], ag[2]), swap32_sum(ag[1], ag[3]));
      float tu = swap16_sum(swap32_sum(au[0], au[2]), swap32_sum(au[1], au[3]));
      tg += dpp<ROR8>(tg); tu += dpp<ROR8>(tu);
      store_granule(p.ws, WS_G1 + ((size_t)((cg * 4 + rb) * 4 + bb) * 64 + ((lane >> 3) & 1) * 32 + 4 * (lane & 7) + r) * 8, tag0, (lane & 8) ? tu : tg);
    }
  }
  int gi = (int)min(claim(WS_GC, GLU_AT), (unsigned)NGLU);
  if (lane == 0) ((int*)lds)[L_TAG + 2] = gi;
  __syncthreads();
  __syncthreads();
  if (gi < NGLU) __syncthreads();
  {
    const size_t gbase = (size_t)(b * 64 + pidx) * 64 * 64 + lane * 8;
    float ya[8], yg[6], mm = 0.f, ll = 0.f;
    unsigned pend = 0x33fffu;
    for (unsigned spins = 0; pend != 0u && spins < (1u << 22); ++spins) {
      unsigned long long ga[8], gg[6], gm_ = 0, gl_ = 0;
#pragma unroll
      for (int jj = 0; jj < 8; ++jj) { ga[jj] = 0; if (pend & (1u << jj)) ga[jj] = load_granule(p.ws, WS_YA + gbase + jj * 512); }
#pragma unroll
      for (int jj = 0; jj < 6; ++jj) { gg[jj] = 0; if (pend & (256u << jj)) gg[jj] = load_granule(p.ws, WS_YG + gbase + jj * 512); }
      if (pend & (1u << 16)) gm_ = load_granule(p.ws, WS_ML + ((size_t)(b * 2) * 64 + lane) * 8);
      if (pend & (1u << 17)) gl_ = load_granule(p.ws, WS_ML + ((size_t)(b * 2 + 1) * 64 + lane) * 8);
#pragma unroll
      for (int jj = 0; jj < 8; ++jj)
        if ((pend & (1u << jj)) && __all((unsigned)(ga[jj] >> 32) == tag)) { ya[jj] = __uint_as_float((unsigned)ga[jj]); pend &= ~(1u << jj); }
#pragma unroll
      for (int jj = 0; jj < 6; ++jj) {
        const bool live = 8 * jj + (lane >> 3) < NGLU;
        if ((pend & (256u << jj)) && __all(!live || (unsigned)(gg[jj] >> 32) == tag)) { yg[jj] = live ? __uint_as_float((unsigned)gg[jj]) : 0.f; pend &= ~(256u << jj); }
      }
      if ((pend & (1u << 16)) && __all((unsigned)(gm_ >> 32) == tag)) { mm = __uint_as_float((unsigned)gm_); pend &= ~(1u << 16); }
      if ((pend & (1u << 17)) && __all((unsigned)(gl_ >> 32) == tag)) { ll = __uint_as_float((unsigned)gl_); pend &= ~(1u << 17); }
      if (pend) __builtin_amdgcn_s_sleep(1);
    }
    const float mo = dpp<0xB1>(mm), lo_ = dpp<0xB1>(ll);
    const float M = fmaxf(mm, mo);
    const float w0 = __builtin_amdgcn_exp2f(mm - M), w1 = __builtin_amdgcn_exp2f(mo - M);
    const float scale = gate * w0 / fmaf(w0, ll, w1 * lo_);
    float accA = 0.f, accG = 0.f;
#pragma unroll
    for (int jj = 0; jj < 8; ++jj) accA = fmaf(__shfl(scale, 8 * jj + (lane >> 3)), ya[jj], accA);
#pragma unroll
    for (int jj = 0; jj < 6; ++jj) accG += yg[jj];
    float o = fmaf(1.f - gate, accG, accA);
    o += dpp<ROR8>(o);
    o = xrow_sum(o);
    if (pend) o = __builtin_nanf("");
    const unsigned short ob = __builtin_bit_cast(unsigned short, (__bf16)o);
    if (lane < 8) p.out[b * DM + 8 * pidx + lane] = ob;
    if (pidx == 0 && lane == 0) __hip_atomic_store((gu32*)(p.ws + WS_E + b * 256), tag, RLX_AGENT);
    STAMP(7);
  }
}

}

extern "C" void kernel_launch(void* const* d_in, const int* in_sizes, int n_in, void* d_out, int out_size, void* d_ws,
                              size_t ws_size, hipStream_t stream) {
  (void)in_sizes; (void)n_in; (void)out_size; (void)ws_size;
  Params p;
  p.x = (const unsigned short*)d_in[0]; p.k = (const unsigned short*)d_in[1]; p.v = (const unsigned short*)d_in[2];
  p.wq = (const unsigned short*)d_in[3]; p.wo = (const unsigned short*)d_in[4];
  p.wg = (const unsigned short*)d_in[6]; p.wu = (const unsigned short*)d_in[7]; p.wd = (const unsigned short*)d_in[8];
  p.gw = (const unsigned short*)d_in[9];
  p.out = (unsigned short*)d_out; p.ws = (unsigned char*)d_ws;
  hipLaunchKernelGGL(fused_decode_kernel, dim3(NWG), dim3(NTHR), 0, stream, p);
}
```

```cpp
#include <hip/hip_runtime.h>
#include <hip/hip_bf16.h>
#include <stdint.h>

typedef unsigned u32x4 __attribute__((ext_vector_type(4)));
typedef unsigned u32x2 __attribute__((ext_vector_type(2)));
typedef float f32x2 __attribute__((ext_vector_type(2)));
typedef float f32x4 __attribute__((ext_vector_type(4)));
typedef __bf16 bf16x2 __attribute__((ext_vector_type(2)));
#define GAS __attribute__((address_space(1)))
#define LAS __attribute__((address_space(3)))
typedef GAS unsigned gu32;
typedef GAS unsigned long long gu64;
#define RLX_AGENT __ATOMIC_RELAXED, __HIP_MEMORY_SCOPE_AGENT

namespace {

constexpr int DM = 512, NH = 32, SK = 4096, DH = 128, FF = 1376;
constexpr int NWG = 256, NSW = 8, NWAVE = NSW + 1, NTHR = NWAVE * 64;
constexpr int KEYS_WG = SK / 2;
constexpr int ITEM_KEYS = 16;
constexpr int NITEM = KEYS_WG / (ITEM_KEYS * NSW);
constexpr unsigned SLICE_BYTES = KEYS_WG * DH * 2;
constexpr int NGLU = FF / 32;

constexpr size_t WS_E = 0;
constexpr size_t WS_GC = 2048;
constexpr size_t WS_ML = 4096;
constexpr size_t WS_YA = 65536;
constexpr size_t WS_YG = WS_YA + 4 * 64 * 64 * 64;
constexpr size_t WS_G1 = WS_YG + 4 * 64 * 64 * 64;
constexpr size_t WS_END = WS_G1 + 43 * 4 * 4 * 64 * 8;

#define AUX_K_ 0
#define AUX_V_ 2
#define AUX_W_ 0
constexpr int AUX_K = AUX_K_, AUX_V = AUX_V_, AUX_W = AUX_W_;
#define WQ_BARRIER 1
#define MODE_B 0
#define X_GU(x) (x)
#define X_WD(x) (x)
#define X_ST 1
#define G1_AT 9
#define GLU_AT 14
#define NPRE_ 1
constexpr int NPRE = NPRE_;
#define STAMP(k) do {} while (0)
#define STAMP0(k) do {} while (0)

__device__ __forceinline__ float dot2(unsigned a, unsigned b, float c) {
  return __builtin_amdgcn_fdot2_f32_bf16(__builtin_bit_cast(bf16x2, a), __builtin_bit_cast(bf16x2, b), c, false);
}
template <int CTRL> __device__ __forceinline__ float dpp(float x) {
  return __builtin_bit_cast(float, __builtin_amdgcn_mov_dpp(__builtin_bit_cast(int, x), CTRL, 0xf, 0xf, true));
}
constexpr int ROR8 = 0x128, ROR4 = 0x124, ROR2 = 0x122, ROR1 = 0x121;
__device__ __forceinline__ float row_allsum(float x) {
  x += dpp<ROR8>(x); x += dpp<ROR4>(x); x += dpp<ROR2>(x); x += dpp<ROR1>(x); return x;
}
__device__ __forceinline__ float swap32_sum(float a, float b) {
  auto s = __builtin_amdgcn_permlane32_swap(__float_as_uint(a), __float_as_uint(b), false, false);
  return __uint_as_float(s[0]) + __uint_as_float(s[1]);
}
__device__ __forceinline__ float swap16_sum(float a, float b) {
  auto s = __builtin_amdgcn_permlane16_swap(__float_as_uint(a), __float_as_uint(b), false, false);
  return __uint_as_float(s[0]) + __uint_as_float(s[1]);
}
__device__ __forceinline__ float xrow_max(float x) {
  auto s = __builtin_amdgcn_permlane16_swap(__float_as_uint(x), __float_as_uint(x), false, false);
  x = fmaxf(__uint_as_float(s[0]), __uint_as_float(s[1]));
  auto t = __builtin_amdgcn_permlane32_swap(__float_as_uint(x), __float_as_uint(x), false, false);
  return fmaxf(__uint_as_float(t[0]), __uint_as_float(t[1]));
}
__device__ __forceinline__ float xrow_sum(float x) { x = swap16_sum(x, x); return swap32_sum(x, x); }
__device__ __forceinline__ f32x2 xrow_treduce8(const float (&a)[8]) {
  float t0 = swap32_sum(a[0], a[4]), t1 = swap32_sum(a[1], a[5]), t2 = swap32_sum(a[2], a[6]), t3 = swap32_sum(a[3], a[7]);
  f32x2 r; r.x = swap16_sum(t0, t2); r.y = swap16_sum(t1, t3); return r;
}
__device__ __forceinline__ float bf_lo(unsigned w) { return __uint_as_float(w << 16); }
__device__ __forceinline__ float bf_hi(unsigned w) { return __uint_as_float(w & 0xffff0000u); }
__device__ __forceinline__ unsigned pack_bf16(float lo, float hi) {
  bf16x2 v; v.x = (__bf16)lo; v.y = (__bf16)hi; return __builtin_bit_cast(unsigned, v);
}
__device__ __forceinline__ float sigmoidf_(float z) { return 1.f / (1.f + __expf(-z)); }
__device__ __forceinline__ void store_granule(unsigned char* ws, size_t off, unsigned tag, float v) {
  __hip_atomic_store((gu64*)(ws + off), ((unsigned long long)tag << 32) | __float_as_uint(v), RLX_AGENT);
}
__device__ __forceinline__ unsigned long long load_granule(const unsigned char* ws, size_t off) {
  return __hip_atomic_load((gu64*)(ws + off), RLX_AGENT);
}
__device__ __forceinline__ void axpy8(float (&acc)[8], const u32x4& w, unsigned xl, unsigned xh) {
#pragma unroll
  for (int k = 0; k < 4; ++k) { acc[2 * k] = dot2(w[k], xl, acc[2 * k]); acc[2 * k + 1] = dot2(w[k], xh, acc[2 * k + 1]); }
}

struct Params {
  const unsigned short* x; const unsigned short* k; const unsigned short* v; const unsigned short* wq; const unsigned short* wo;
  const unsigned short* wg; const unsigned short* wu; const unsigned short* wd; const unsigned short* gw;
  unsigned short* out; unsigned char* ws;
};

constexpr int L_WO = 0;
constexpr int L_QP = L_WO + 128 * 256;
constexpr int L_ML = L_QP + NSW * 128;
constexpr int L_TAG = L_ML + 16;
constexpr int L_Y = L_TAG + 16;
constexpr int L_END = L_Y + NSW * 512;

constexpr float C2 = 0.08838834764831845f * 1.4426950408889634f;

#define PIN8(a) asm volatile("" : "+v"(a[0]), "+v"(a[1]), "+v"(a[2]), "+v"(a[3]), "+v"(a[4]), "+v"(a[5]), "+v"(a[6]), "+v"(a[7]))
#define SB() __builtin_amdgcn_sched_barrier(0)

__device__ __forceinline__ float qk_dot(const u32x4& k, const u32x4& q) {
  float d = dot2(k[0], q[0], 0.f);
  d = dot2(k[1], q[1], d); d = dot2(k[2], q[2], d); return dot2(k[3], q[3], d);
}
__device__ __forceinline__ void softmax_step(const float (&sc)[4], float& m, float& l, float (&acc)[8], float (&pr)[4]) {
  const float mn = fmaxf(fmaxf(fmaxf(sc[0], sc[1]), fmaxf(sc[2], sc[3])), m);
  const float alpha = __builtin_amdgcn_exp2f(m - mn);
  m = mn;
#pragma unroll
  for (int u = 0; u < 4; ++u) pr[u] = __builtin_amdgcn_exp2f(sc[u] - mn);
  l = fmaf(l, alpha, (pr[0] + pr[1]) + (pr[2] + pr[3]));
#pragma unroll
  for (int k = 0; k < 8; ++k) acc[k] *= alpha;
}
__device__ __forceinline__ void pv_step(float (&acc)[8], float p, const u32x4& v) {
#pragma unroll
  for (int k = 0; k < 4; ++k) { acc[2 * k] = fmaf(p, bf_lo(v[k]), acc[2 * k]); acc[2 * k + 1] = fmaf(p, bf_hi(v[k]), acc[2 * k + 1]); }
}

__global__ void __launch_bounds__(NTHR, 3) fused_decode_kernel(Params p) {
  __shared__ __attribute__((aligned(16))) float lds[L_END];
  const int tid = threadIdx.x, lane = tid & 63, wave = __builtin_amdgcn_readfirstlane(tid >> 6);
  const int r = lane >> 4, c = lane & 15;
  const int bid = blockIdx.x, grp = bid & 7, j = bid >> 3;
  const int h = grp * 4 + (j >> 3), b = (j >> 1) & 3, s = j & 1;
  const int pidx = 2 * h + s;
  const unsigned lo16 = lane * 16;
  STAMP(0);

  if (wave < NSW) {
    const __amdgpu_buffer_rsrc_t rq = __builtin_amdgcn_make_buffer_rsrc((void*)p.wq, 0, DM * NH * DH * 2, 0x00020000);
    const u32x4* xq_p = (const u32x4*)(p.x + b * DM + 64 * wave + 16 * r);
    const u32x4 xq0 = xq_p[0], xq1 = xq_p[1];
    u32x4 wreg[16];
    {
      const unsigned vo = ((64 * wave + 16 * r) * (NH * DH) + h * DH + 8 * c) * 2;
#pragma unroll
      for (int i = 0; i < 16; ++i) wreg[i] = __builtin_amdgcn_raw_buffer_load_b128(rq, vo, i * (NH * DH * 2), AUX_W);
    }
    SB();
    const size_t slice = ((size_t)(b * NH + h) * SK + (size_t)s * KEYS_WG) * DH;
    const __amdgpu_buffer_rsrc_t rk = __builtin_amdgcn_make_buffer_rsrc((void*)(p.k + slice), 0, SLICE_BYTES, 0x00020000);
    const __amdgpu_buffer_rsrc_t rv = __builtin_amdgcn_make_buffer_rsrc((void*)(p.v + slice), 0, SLICE_BYTES, 0x00020000);
    const __amdgpu_buffer_rsrc_t ro = __builtin_amdgcn_make_buffer_rsrc((void*)(p.wo + (size_t)(h * DH + 16 * wave) * DM), 0, 16 * DM * 2, 0x00020000);
    __builtin_amdgcn_s_barrier();
    u32x4 pk[NPRE][4], pv[NPRE][4];
    unsigned soff = wave * 4096;
#pragma unroll
    for (int n = 0; n < NPRE; ++n) {
#pragma unroll
      for (int u = 0; u < 4; ++u) pk[n][u] = __builtin_amdgcn_raw_buffer_load_b128(rk, lo16 + u * 1024, soff + n * (NSW * 4096), AUX_K);
#pragma unroll
      for (int u = 0; u < 4; ++u) pv[n][u] = __builtin_amdgcn_raw_buffer_load_b128(rv, lo16 + u * 1024, soff + n * (NSW * 4096), AUX_V);
    }
    SB();
    {
      float acc[8];
#pragma unroll
      for (int k = 0; k < 8; ++k) acc[k] = 0.f;
#pragma unroll
      for (int i = 0; i < 16; ++i) {
        const unsigned d = i < 8 ? xq0[(i >> 1) & 3] : xq1[(i >> 1) & 3];
        const unsigned xl = (i & 1) ? (d >> 16) : (d & 0xffffu), xh = (i & 1) ? (d & 0xffff0000u) : (d << 16);
        axpy8(acc, wreg[i], xl, xh);
      }
      const f32x2 t = xrow_treduce8(acc);
      *(f32x2*)(lds + L_QP + wave * 128 + 8 * c + 2 * r) = t;
    }
    SB();
    soff += NPRE * (NSW * 4096);
    u32x4 a[4];
#pragma unroll
    for (int u = 0; u < 4; ++u) a[u] = __builtin_amdgcn_raw_buffer_load_b128(rk, lo16 + u * 1024, soff, AUX_K);
    u32x4 wo_r = __builtin_amdgcn_raw_buffer_load_b128(ro, lo16, 0, AUX_W);
    SB();
    __syncthreads();
    STAMP(1);
    u32x4 qp;
    {
      float qa[8];
      const float* q0 = lds + L_QP + (2 * r) * 128 + 8 * c;
      const f32x4 x0 = *(const f32x4*)(q0), x1 = *(const f32x4*)(q0 + 4), y0 = *(const f32x4*)(q0 + 128), y1 = *(const f32x4*)(q0 + 132);
#pragma unroll
      for (int k = 0; k < 4; ++k) { qa[k] = xrow_sum(x0[k] + y0[k]); qa[k + 4] = xrow_sum(x1[k] + y1[k]); }
#pragma unroll
      for (int k = 0; k < 4; ++k) qp[k] = pack_bf16(qa[2 * k], qa[2 * k + 1]);
    }
    float m = -INFINITY, l = 0.f, acc[8];
#pragma unroll
    for (int k = 0; k < 8; ++k) acc[k] = 0.f;
#pragma unroll
    for (int n = 0; n < NPRE; ++n) {
      float sc[4], pr[4];
#pragma unroll
      for (int u = 0; u < 4; ++u) sc[u] = row_allsum(qk_dot(pk[n][u], qp)) * C2;
      softmax_step(sc, m, l, acc, pr);
#pragma unroll
      for (int u = 0; u < 4; ++u) pv_step(acc, pr[u], pv[n][u]);
      PIN8(acc); SB();
    }
    u32x4* wo_lds = (u32x4*)(lds + L_WO) + wave * 1024 + lane;
#pragma unroll 1
    for (int it = NPRE; it < NITEM; ++it) {
      wo_lds[(it - 1) * 64] = wo_r;
      if (wave == 0) __hip_atomic_store((LAS unsigned*)lds + L_TAG + 1, (unsigned)it, __ATOMIC_RELAXED, __HIP_MEMORY_SCOPE_WORKGROUP);
      SB();
      float sc[4], pr[4];
#pragma unroll
      for (int u = 0; u < 4; ++u) {
        const float d = qk_dot(a[u], qp);
        SB();
        a[u] = __builtin_amdgcn_raw_buffer_load_b128(rv, lo16 + u * 1024, soff, AUX_V);
        SB();
        sc[u] = row_allsum(d) * C2;
      }
      wo_r = __builtin_amdgcn_raw_buffer_load_b128(ro, lo16, it * 1024, AUX_W);
      softmax_step(sc, m, l, acc, pr);
      soff += NSW * 4096;
      SB();
#pragma unroll
      for (int u = 0; u < 4; ++u) {
        pv_step(acc, pr[u], a[u]);
        PIN8(acc); SB();
        a[u] = __builtin_amdgcn_raw_buffer_load_b128(rk, lo16 + u * 1024, soff, AUX_K);
        SB();
      }
    }
    STAMP(3);
#pragma unroll
    for (int i = 0; i < 15; ++i) wreg[i] = wo_lds[i * 64];
    wreg[15] = wo_r;
    {
      const float mw = xrow_max(m);
      const float sw = __builtin_amdgcn_exp2f(m - mw);
#pragma unroll
      for (int k = 0; k < 8; ++k) acc[k] *= sw;
      const float lw = xrow_sum(l * sw);
      const f32x2 t = xrow_treduce8(acc);
      *(f32x2*)(lds + L_QP + wave * 128 + 8 * c + 2 * r) = t;
      if (lane == 0) *(f32x2*)(lds + L_ML + 2 * wave) = f32x2{mw, lw};
    }
    __syncthreads();
    const unsigned tag = __builtin_amdgcn_readfirstlane(((const unsigned*)lds)[L_TAG]);
    const int gi = __builtin_amdgcn_readfirstlane(((const int*)lds)[L_TAG + 2]);
    u32x4 wdr[4];
    unsigned long long gg = 0, gu = 0;
    const size_t g1off = WS_G1 + ((size_t)((gi * 4 + r) * 4 + (c & 3)) * 64 + 4 * wave + (c >> 2)) * 8;
    STAMP(4);
    if (gi < NGLU) {
      const __amdgpu_buffer_rsrc_t rd = __builtin_amdgcn_make_buffer_rsrc((void*)(p.wd + (size_t)(gi * 32 + 4 * wave) * DM), 0, 4 * DM * 2, 0x00020000);
      gg = load_granule(p.ws, g1off); gu = load_granule(p.ws, g1off + 256);
#pragma unroll
      for (int i = 0; i < 4; ++i) wdr[i] = __builtin_amdgcn_raw_buffer_load_b128(rd, lo16, i * 1024, AUX_W);
    }
    SB();
    {
      float mv[8], lv[8];
#pragma unroll
      for (int w = 0; w < 8; w += 2) { const f32x4 t = *(const f32x4*)(lds + L_ML + 2 * w); mv[w] = t[0]; lv[w] = t[1]; mv[w + 1] = t[2]; lv[w + 1] = t[3]; }
      float M = mv[0];
#pragma unroll
      for (int w = 1; w < 8; ++w) M = fmaxf(M, mv[w]);
      const f32x4 mine = *(const f32x4*)(lds + L_ML + 4 * r);
      const float s0 = __builtin_amdgcn_exp2f(mine[0] - M), s1 = __builtin_amdgcn_exp2f(mine[2] - M);
      float o = s0 * lds[L_QP + (2 * r) * 128 + 16 * wave + c] + s1 * lds[L_QP + (2 * r + 1) * 128 + 16 * wave + c];
      o = xrow_sum(o);
      float y[8];
#pragma unroll
      for (int k = 0; k < 8; ++k) y[k] = 0.f;
#pragma unroll
      for (int i = 0; i < 16; ++i) {
        const float oi = __builtin_bit_cast(float, __builtin_amdgcn_readlane(__builtin_bit_cast(int, o), i));
        pv_step(y, oi, wreg[i]);
        if (i & 1) { PIN8(y); SB(); }
      }
      *(f32x4*)(lds + L_Y + wave * 512 + 8 * lane) = f32x4{y[0], y[1], y[2], y[3]};
      *(f32x4*)(lds + L_Y + wave * 512 + 8 * lane + 4) = f32x4{y[4], y[5], y[6], y[7]};
      if (wave == 6) {
        float L = 0.f;
#pragma unroll
        for (int w = 0; w < 8; ++w) L = fmaf(__builtin_amdgcn_exp2f(mv[w] - M), lv[w], L);
        if (lane < 2) store_granule(p.ws, WS_ML + ((size_t)(b * 2 + lane) * 64 + pidx) * 8, tag, lane ? L : M);
      }
    }
    __syncthreads();
    STAMP(5);
    {
      float y = 0.f;
#pragma unroll
      for (int w = 0; w < 8; ++w) y += lds[L_Y + w * 512 + tid];
      store_granule(p.ws, WS_YA + ((size_t)((b * 64 + (tid >> 3)) * 64 + pidx) * 8 + (tid & 7)) * 8, tag, y);
    }
    STAMP(6);
    if (gi >= NGLU) return;
    float* gp = lds + L_WO;
    {
      const unsigned tag0 = ((const unsigned*)lds)[L_TAG + 4];
      for (unsigned spins = 0; spins < (1u << 22); ++spins) {
        if (__all((unsigned)(gg >> 32) == tag0 && (unsigned)(gu >> 32) == tag0)) break;
        __builtin_amdgcn_s_sleep(4);
        gg = load_granule(p.ws, g1off); gu = load_granule(p.ws, g1off + 256);
      }
      const float gs = xrow_sum(__uint_as_float((unsigned)gg)), us = xrow_sum(__uint_as_float((unsigned)gu));
      const float hv = sigmoidf_(gs) * us;
      float* yp = gp + 4096 + wave * 2048 + 8 * lane;
#pragma unroll
      for (int bb = 0; bb < 4; ++bb) {
        float y[8];
#pragma unroll
        for (int k = 0; k < 8; ++k) y[k] = 0.f;
#pragma unroll
        for (int i = 0; i < 4; ++i)
          pv_step(y, __builtin_bit_cast(float, __builtin_amdgcn_readlane(__builtin_bit_cast(int, hv), 4 * i + bb)), wdr[i]);
        *(f32x4*)(yp + bb * 512) = f32x4{y[0], y[1], y[2], y[3]};
        *(f32x4*)(yp + bb * 512 + 4) = f32x4{y[4], y[5], y[6], y[7]};
      }
    }
    __syncthreads();
#pragma unroll
    for (int bb = 0; bb < 4; ++bb) {
      float y = 0.f;
#pragma unroll
      for (int w = 0; w < 8; ++w) y += gp[4096 + w * 2048 + bb * 512 + tid];
      store_granule(p.ws, WS_YG + ((size_t)((bb * 64 + (tid >> 3)) * 64 + gi) * 8 + (tid & 7)) * 8, ((const unsigned*)lds)[L_TAG + 4 + bb], y);
    }
    STAMP(2);
    return;
  }

  const unsigned tags = __hip_atomic_load((gu32*)(p.ws + WS_E + (lane & 3) * 256), RLX_AGENT) + 1u;
  __builtin_amdgcn_s_barrier();
  SB();
  const unsigned tag = __builtin_amdgcn_readlane(tags, b);
  if (lane == 0) { ((unsigned*)lds)[L_TAG] = tag; ((unsigned*)lds)[L_TAG + 1] = 0u; }
  if (lane < 4) ((unsigned*)lds)[L_TAG + 4 + lane] = tags;
  if (bid == 0 && lane < 2) __hip_atomic_store((gu64*)(p.ws + WS_GC + lane * 256), (unsigned long long)tag << 32, RLX_AGENT);
  __syncthreads();
  float gate;
  {
    const u32x4 xg = *(const u32x4*)(p.x + b * DM + 8 * lane), gw = *(const u32x4*)(p.gw + 8 * lane);
    float z = 0.f;
#pragma unroll
    for (int k = 0; k < 4; ++k) z = dot2(xg[k], gw[k], z);
    gate = sigmoidf_(xrow_sum(row_allsum(z)));
  }
  const unsigned tag0 = __builtin_amdgcn_readlane(tags, 0);
  auto claim = [&](size_t off, unsigned at) -> unsigned {
    while (__hip_atomic_load((LAS unsigned*)lds + L_TAG + 1, __ATOMIC_RELAXED, __HIP_MEMORY_SCOPE_WORKGROUP) < at) __builtin_amdgcn_s_sleep(8);
#pragma unroll 1
    for (unsigned tries = 0; tries < 65536u; ++tries) {
      unsigned long long tk = 0;
      if (lane == 0) tk = __hip_atomic_fetch_add((gu64*)(p.ws + off), 1ull, RLX_AGENT);
      if (__builtin_amdgcn_readfirstlane((unsigned)(tk >> 32)) == tag0) return __builtin_amdgcn_readfirstlane((unsigned)tk);
      __builtin_amdgcn_s_sleep(32);
    }
    return 0xffffu;
  };
  const unsigned t1 = claim(WS_GC + 256, G1_AT);
  if (t1 < 4u * NGLU) {
    const int cg = t1 >> 2, rb = t1 & 3, row0 = 128 * rb + 16 * (lane >> 3);
    const __amdgpu_buffer_rsrc_t rg = __builtin_amdgcn_make_buffer_rsrc((void*)p.wg, 0, DM * FF * 2, 0x00020000);
    const __amdgpu_buffer_rsrc_t ru = __builtin_amdgcn_make_buffer_rsrc((void*)p.wu, 0, DM * FF * 2, 0x00020000);
    const unsigned vo = (row0 * FF + 32 * cg + 4 * (lane & 7)) * 2;
    u32x4 xr[4][2];
#pragma unroll
    for (int bb = 0; bb < 4; ++bb) { xr[bb][0] = *(const u32x4*)(p.x + bb * DM + row0); xr[bb][1] = *(const u32x4*)(p.x + bb * DM + row0 + 8); }
    u32x2 wgr[16], wur[16];
#pragma unroll
    for (int i = 0; i < 16; ++i) { wgr[i] = __builtin_amdgcn_raw_buffer_load_b64(rg, vo, i * (FF * 2), AUX_W); wur[i] = __builtin_amdgcn_raw_buffer_load_b64(ru, vo, i * (FF * 2), AUX_W); }
    SB();
#pragma unroll 1
    for (int bb = 0; bb < 4; ++bb) {
      const u32x4 x0 = bb == 0 ? xr[0][0] : bb == 1 ? xr[1][0] : bb == 2 ? xr[2][0] : xr[3][0];
      const u32x4 x1 = bb == 0 ? xr[0][1] : bb == 1 ? xr[1][1] : bb == 2 ? xr[2][1] : xr[3][1];
      float ag[4] = {0.f, 0.f, 0.f, 0.f}, au[4] = {0.f, 0.f, 0.f, 0.f};
#pragma unroll
      for (int i = 0; i < 16; ++i) {
        const unsigned d = i < 8 ? x0[(i >> 1) & 3] : x1[(i >> 1) & 3];
        const unsigned xl = (i & 1) ? (d >> 16) : (d & 0xffffu), xh = (i & 1) ? (d & 0xffff0000u) : (d << 16);
#pragma unroll
        for (int k = 0; k < 2; ++k) {
          ag[2 * k] = dot2(wgr[i][k], xl, ag[2 * k]); ag[2 * k + 1] = dot2(wgr[i][k], xh, ag[2 * k + 1]);
          au[2 * k] = dot2(wur[i][k], xl, au[2 * k]); au[2 * k + 1] = dot2(wur[i][k], xh, au[2 * k + 1]);
        }
      }
      float tg = swap16_sum(swap32_sum(ag[0], ag[2]), swap32_sum(ag[1], ag[3]));
      float tu = swap16_sum(swap32_sum(au[0], au[2]), swap32_sum(au[1], au[3]));
      tg += dpp<ROR8>(tg); tu += dpp<ROR8>(tu);
      store_granule(p.ws, WS_G1 + ((size_t)((cg * 4 + rb) * 4 + bb) * 64 + ((lane >> 3) & 1) * 32 + 4 * (lane & 7) + r) * 8, tag0, (lane & 8) ? tu : tg);
    }
  }
  int gi = (int)min(claim(WS_GC, GLU_AT), (unsigned)NGLU);
  if (lane == 0) ((int*)lds)[L_TAG + 2] = gi;
  __syncthreads();
  __syncthreads();
  if (gi < NGLU) __syncthreads();
  {
    const size_t gbase = (size_t)(b * 64 + pidx) * 64 * 64 + lane * 8;
    float ya[8], yg[6], mm = 0.f, ll = 0.f;
    unsigned pend = 0x33fffu;
    for (unsigned spins = 0; pend != 0u && spins < (1u << 22); ++spins) {
      unsigned long long ga[8], gg[6], gm_ = 0, gl_ = 0;
#pragma unroll
      for (int jj = 0; jj < 8; ++jj) { ga[jj] = 0; if (pend & (1u << jj)) ga[jj] = load_granule(p.ws, WS_YA + gbase + jj * 512); }
#pragma unroll
      for (int jj = 0; jj < 6; ++jj) { gg[jj] = 0; if (pend & (256u << jj)) gg[jj] = load_granule(p.ws, WS_YG + gbase + jj * 512); }
      if (pend & (1u << 16)) gm_ = load_granule(p.ws, WS_ML + ((size_t)(b * 2) * 64 + lane) * 8);
      if (pend & (1u << 17)) gl_ = load_granule(p.ws, WS_ML + ((size_t)(b * 2 + 1) * 64 + lane) * 8);
#pragma unroll
      for (int jj = 0; jj < 8; ++jj)
        if ((pend & (1u << jj)) && __all((unsigned)(ga[jj] >> 32) == tag)) { ya[jj] = __uint_as_float((unsigned)ga[jj]); pend &= ~(1u << jj); }
#pragma unroll
      for (int jj = 0; jj < 6; ++jj) {
        const bool live = 8 * jj + (lane >> 3) < NGLU;
        if ((pend & (256u << jj)) && __all(!live || (unsigned)(gg[jj] >> 32) == tag)) { yg[jj] = live ? __uint_as_float((unsigned)gg[jj]) : 0.f; pend &= ~(256u << jj); }
      }
      if ((pend & (1u << 16)) && __all((unsigned)(gm_ >> 32) == tag)) { mm = __uint_as_float((unsigned)gm_); pend &= ~(1u << 16); }
      if ((pend & (1u << 17)) && __all((unsigned)(gl_ >> 32) == tag)) { ll = __uint_as_float((unsigned)gl_); pend &= ~(1u << 17); }
      if (pend) __builtin_amdgcn_s_sleep(1);
    }
    const float mo = dpp<0xB1>(mm), lo_ = dpp<0xB1>(ll);
    const float M = fmaxf(mm, mo);
    const float w0 = __builtin_amdgcn_exp2f(mm - M), w1 = __builtin_amdgcn_exp2f(mo - M);
    const float scale = gate * w0 / fmaf(w0, ll, w1 * lo_);
    float accA = 0.f, accG = 0.f;
#pragma unroll
    for (int jj = 0; jj < 8; ++jj) accA = fmaf(__shfl(scale, 8 * jj + (lane >> 3)), ya[jj], accA);
#pragma unroll
    for (int jj = 0; jj < 6; ++jj) accG += yg[jj];
    float o = fmaf(1.f - gate, accG, accA);
    o += dpp<ROR8>(o);
    o = xrow_sum(o);
    if (pend) o = __builtin_nanf("");
    const unsigned short ob = __builtin_bit_cast(unsigned short, (__bf16)o);
    if (lane < 8) p.out[b * DM + 8 * pidx + lane] = ob;
    if (pidx == 0 && lane == 0) __hip_atomic_store((gu32*)(p.ws + WS_E + b * 256), tag, RLX_AGENT);
    STAMP(7);
  }
}

}

extern "C" void kernel_launch(void* const* d_in, const int* in_sizes, int n_in, void* d_out, int out_size, void* d_ws,
                              size_t ws_size, hipStream_t stream) {
  (void)in_sizes; (void)n_in; (void)out_size; (void)ws_size;
  Params p;
  p.x = (const unsigned short*)d_in[0]; p.k = (const unsigned short*)d_in[1]; p.v = (const unsigned short*)d_in[2];
  p.wq = (const unsigned short*)d_in[3]; p.wo = (const unsigned short*)d_in[4];
  p.wg = (const unsigned short*)d_in[6]; p.wu = (const unsigned short*)d_in[7]; p.wd = (const unsigned short*)d_in[8];
  p.gw = (const unsigned short*)d_in[9];
  p.out = (unsigned short*)d_out; p.ws = (unsigned char*)d_ws;
  hipLaunchKernelGGL(fused_decode_kernel, dim3(NWG), dim3(NTHR), 0, stream, p);
}
```

```cpp
#include <hip/hip_runtime.h>
#include <hip/hip_bf16.h>
#include <stdint.h>

typedef unsigned u32x4 __attribute__((ext_vector_type(4)));
typedef unsigned u32x2 __attribute__((ext_vector_type(2)));
typedef float f32x2 __attribute__((ext_vector_type(2)));
typedef float f32x4 __attribute__((ext_vector_type(4)));
typedef __bf16 bf16x2 __attribute__((ext_vector_type(2)));
typedef __bf16 bf16x8 __attribute__((ext_vector_type(8)));
#define GAS __attribute__((address_space(1)))
#define LAS __attribute__((address_space(3)))
typedef GAS unsigned gu32;
typedef GAS unsigned long long gu64;
#define RLX_AGENT __ATOMIC_RELAXED, __HIP_MEMORY_SCOPE_AGENT
#define RLX_WG __ATOMIC_RELAXED, __HIP_MEMORY_SCOPE_WORKGROUP

namespace {

constexpr int DM = 512, NH = 32, SK = 4096, DH = 128, FF = 1376;
constexpr int NWG = 256, NSW = 8, NWAVE = NSW + 1, NTHR = NWAVE * 64;
constexpr int KEYS_WG = SK / 2;
constexpr int NITEM = KEYS_WG / (16 * NSW);
constexpr unsigned SLICE_BYTES = KEYS_WG * DH * 2;
constexpr int NGLU = FF / 32;

constexpr size_t WS_E = 0;
constexpr size_t WS_ML = 4096;
constexpr size_t WS_YA = 65536;
constexpr size_t WS_YG = WS_YA + 4 * 64 * 64 * 64;
constexpr size_t WS_G1 = WS_YG + 4 * 64 * 64 * 64;

constexpr int AUX_NT = 2;
constexpr int AUX_W = 0;
constexpr unsigned SPIN_CAP = 1u << 20;
constexpr int NVE = 2, NV0 = 3;
constexpr unsigned PACE_K = 244, PACE_V = 236, T0 = 1600;
constexpr unsigned G1_T0 = 800, G1_GAP = 160;

__device__ __forceinline__ float dot2(unsigned a, unsigned b, float c) {
  return __builtin_amdgcn_fdot2_f32_bf16(__builtin_bit_cast(bf16x2, a), __builtin_bit_cast(bf16x2, b), c, false);
}
template <int CTRL> __device__ __forceinline__ float dpp(float x) {
  return __builtin_bit_cast(float, __builtin_amdgcn_mov_dpp(__builtin_bit_cast(int, x), CTRL, 0xf, 0xf, true));
}
constexpr int ROR8 = 0x128, ROR4 = 0x124, ROR2 = 0x122, ROR1 = 0x121;
__device__ __forceinline__ float row_allsum(float x) {
  x += dpp<ROR8>(x); x += dpp<ROR4>(x); x += dpp<ROR2>(x); x += dpp<ROR1>(x); return x;
}
__device__ __forceinline__ float swap32_sum(float a, float b) {
  auto s = __builtin_amdgcn_permlane32_swap(__float_as_uint(a), __float_as_uint(b), false, false);
  return __uint_as_float(s[0]) + __uint_as_float(s[1]);
}
__device__ __forceinline__ float swap16_sum(float a, float b) {
  auto s = __builtin_amdgcn_permlane16_swap(__float_as_uint(a), __float_as_uint(b), false, false);
  return __uint_as_float(s[0]) + __uint_as_float(s[1]);
}
__device__ __forceinline__ float xrow_max(float x) {
  auto s = __builtin_amdgcn_permlane16_swap(__float_as_uint(x), __float_as_uint(x), false, false);
  x = fmaxf(__uint_as_float(s[0]), __uint_as_float(s[1]));
  auto t = __builtin_amdgcn_permlane32_swap(__float_as_uint(x), __float_as_uint(x), false, false);
  return fmaxf(__uint_as_float(t[0]), __uint_as_float(t[1]));
}
__device__ __forceinline__ float xrow_sum(float x) { x = swap16_sum(x, x); return swap32_sum(x, x); }
__device__ __forceinline__ f32x2 xrow_treduce8(const float (&a)[8]) {
  float t0 = swap32_sum(a[0], a[4]), t1 = swap32_sum(a[1], a[5]), t2 = swap32_sum(a[2], a[6]), t3 = swap32_sum(a[3], a[7]);
  f32x2 r; r.x = swap16_sum(t0, t2); r.y = swap16_sum(t1, t3); return r;
}
__device__ __forceinline__ float bf_lo(unsigned w) { return __uint_as_float(w << 16); }
__device__ __forceinline__ float bf_hi(unsigned w) { return __uint_as_float(w & 0xffff0000u); }
__device__ __forceinline__ unsigned pack_bf16(float lo, float hi) {
  bf16x2 v; v.x = (__bf16)lo; v.y = (__bf16)hi; return __builtin_bit_cast(unsigned, v);
}
__device__ __forceinline__ float rbf(float x) { return (float)(__bf16)x; }
__device__ __forceinline__ float sigmoidf_(float z) { return 1.f / (1.f + __expf(-z)); }
__device__ __forceinline__ void store_granule(unsigned char* ws, size_t off, unsigned tag, float v) {
  __hip_atomic_store((gu64*)(ws + off), ((unsigned long long)tag << 32) | __float_as_uint(v), RLX_AGENT);
}
__device__ __forceinline__ unsigned long long load_granule(const unsigned char* ws, size_t off) {
  return __hip_atomic_load((gu64*)(ws + off), RLX_AGENT);
}
__device__ __forceinline__ size_t y_off(int b, int n, int producer) {
  return ((size_t)((b * 32 + (n >> 4)) * 64 + producer) * 16 + (n & 15)) * 8;
}
__device__ __forceinline__ void axpy8(float (&acc)[8], const u32x4& w, unsigned xl, unsigned xh) {
#pragma unroll
  for (int k = 0; k < 4; ++k) { acc[2 * k] = dot2(w[k], xl, acc[2 * k]); acc[2 * k + 1] = dot2(w[k], xh, acc[2 * k + 1]); }
}

struct Params {
  const unsigned short* x; const unsigned short* k; const unsigned short* v; const unsigned short* wq; const unsigned short* wo;
  const unsigned short* wg; const unsigned short* wu; const unsigned short* wd; const unsigned short* gw;
  unsigned short* out; unsigned char* ws;
};

constexpr int L_WO = 0;
constexpr int L_QP = L_WO + 128 * 256;
constexpr int L_PB = L_QP + NSW * 128;
constexpr int L_ML = L_PB + 16 * 68;
constexpr int L_CTL = L_ML + 16;
constexpr int L_SC = L_CTL + 16;
constexpr int L_S = L_SC + 8;
constexpr int L_END = L_S + 8 * 256;

constexpr float SCALE = 0.08838834764831845f, LOG2E = 1.4426950408889634f;
__device__ __forceinline__ float score_of(float dot) { return rbf(rbf(dot) * SCALE) * LOG2E; }

#define PIN8(a) asm volatile("" : "+v"(a[0]), "+v"(a[1]), "+v"(a[2]), "+v"(a[3]), "+v"(a[4]), "+v"(a[5]), "+v"(a[6]), "+v"(a[7]))
#define SB() __builtin_amdgcn_sched_barrier(0)
#define LDKV(rs, u, so) __builtin_amdgcn_raw_buffer_load_b128(rs, lo16 + (u) * 1024, so, AUX_NT)

__device__ __forceinline__ float qk_dot(const u32x4& k, const u32x4& q) {
  float d = dot2(k[0], q[0], 0.f);
  d = dot2(k[1], q[1], d); d = dot2(k[2], q[2], d); return dot2(k[3], q[3], d);
}
__device__ __forceinline__ void pv_step(float (&acc)[8], float p, const u32x4& v) {
#pragma unroll
  for (int k = 0; k < 4; ++k) { acc[2 * k] = fmaf(p, bf_lo(v[k]), acc[2 * k]); acc[2 * k + 1] = fmaf(p, bf_hi(v[k]), acc[2 * k + 1]); }
}
__device__ __forceinline__ u32x4 wo_pairs(const u32x2& r0, const u32x2& r1) {
  u32x4 o;
  o[0] = __builtin_amdgcn_perm(r1[0], r0[0], 0x05040100u); o[1] = __builtin_amdgcn_perm(r1[0], r0[0], 0x07060302u);
  o[2] = __builtin_amdgcn_perm(r1[1], r0[1], 0x05040100u); o[3] = __builtin_amdgcn_perm(r1[1], r0[1], 0x07060302u);
  return o;
}
constexpr int XOR1 = 0xB1, XOR2 = 0x4E, XOR7 = 0x141;

__global__ void __launch_bounds__(NTHR, 3) fused_decode_kernel(Params p) {
  __shared__ __attribute__((aligned(16))) float lds[L_END];
  unsigned* const ldsu = (unsigned*)lds;
  const int tid = threadIdx.x, lane = tid & 63, wave = __builtin_amdgcn_readfirstlane(tid >> 6);
  const int r = lane >> 4, c = lane & 15;
  const int bid = blockIdx.x, grp = bid & 7, j = bid >> 3;
  const int h = grp * 4 + (j >> 3), b = (j >> 1) & 3, s = j & 1;
  const int pidx = 2 * h + s;
  const unsigned lo16 = lane * 16;
  const int gi = bid;
  const size_t gbase = y_off(b, 8 * pidx + (lane & 7), lane >> 3);

  const unsigned long long t_start = __builtin_amdgcn_s_memrealtime();
  if (__builtin_expect(wave < NSW, 1)) {
    const size_t slice = ((size_t)(b * NH + h) * SK + (size_t)s * KEYS_WG) * DH;
    const __amdgpu_buffer_rsrc_t rk = __builtin_amdgcn_make_buffer_rsrc((void*)(p.k + slice), 0, SLICE_BYTES, 0x00020000);
    const __amdgpu_buffer_rsrc_t rv = __builtin_amdgcn_make_buffer_rsrc((void*)(p.v + slice), 0, SLICE_BYTES, 0x00020000);
    const __amdgpu_buffer_rsrc_t rq = __builtin_amdgcn_make_buffer_rsrc((void*)p.wq, 0, DM * NH * DH * 2, 0x00020000);
    const __amdgpu_buffer_rsrc_t ro = __builtin_amdgcn_make_buffer_rsrc((void*)(p.wo + (size_t)h * DH * DM), 0, DH * DM * 2, 0x00020000);
    u32x4 v0[NV0][4], af[16];
    float acc[8];
    u32x4 pk[4], a[4];
    unsigned soff = wave * 4096;
    u32x4 qp;
    const unsigned wo_vo = ((8 * r) * DM + 64 * wave + 4 * c) * 2;
    u32x2 wo0, wo1;
    const int wrot = 2 * (j & 7);
#define WO_SOFF(n) ((((((n) + wrot) & 15) >> 2) * 32 + (((n) + wrot) & 3) * 2) * 1024)
#define WO_STAGE(n) do { const u32x4 w4_ = wo_pairs(wo0, wo1); unsigned* d_ = wo_lds + ((((n) + wrot) & 15) >> 2) * 1024 + (((n) + wrot) & 3); \
      d_[0] = w4_[0]; d_[256] = w4_[1]; d_[512] = w4_[2]; d_[768] = w4_[3]; } while (0)
    {
      u32x4 wreg[16];
      const int wb = (wave + j) & 7;
      const unsigned wq_vo = ((64 * wb + 16 * r) * (NH * DH) + h * DH + 8 * c) * 2;
      const u32x4* xq_p = (const u32x4*)(p.x + b * DM + 64 * wb + 16 * r);
#pragma unroll
      for (int u = 0; u < 4; ++u) pk[u] = LDKV(rk, u, soff);
      SB();
      const u32x4 xq0 = xq_p[0], xq1 = xq_p[1];
      SB();
#pragma unroll
      for (int i = 0; i < 8; ++i) wreg[i] = __builtin_amdgcn_raw_buffer_load_b128(rq, wq_vo, i * (NH * DH * 2), AUX_W);
      SB();
      soff += NSW * 4096;
#pragma unroll
      for (int u = 0; u < 4; ++u) a[u] = LDKV(rk, u, soff);
      SB();
#pragma unroll
      for (int i = 8; i < 16; ++i) wreg[i] = __builtin_amdgcn_raw_buffer_load_b128(rq, wq_vo, i * (NH * DH * 2), AUX_W);
      SB();
#pragma unroll
      for (int n = 0; n < NVE; ++n)
#pragma unroll
        for (int u = 0; u < 4; ++u) v0[n][u] = LDKV(rv, u, wave * 4096 + n * (NSW * 4096));
      SB();
#pragma unroll
      for (int k = 0; k < 8; ++k) acc[k] = 0.f;
#pragma unroll
      for (int i = 0; i < 16; ++i) {
        const unsigned d = i < 8 ? xq0[(i >> 1) & 3] : xq1[(i >> 1) & 3];
        const unsigned xl = (i & 1) ? (d >> 16) : (d & 0xffffu), xh = (i & 1) ? (d & 0xffff0000u) : (d << 16);
        axpy8(acc, wreg[i], xl, xh);
      }
      const f32x2 t = xrow_treduce8(acc);
      *(f32x2*)(lds + L_QP + wb * 128 + 8 * c + 2 * r) = t;
      SB();
      wo0 = __builtin_amdgcn_raw_buffer_load_b64(ro, wo_vo, WO_SOFF(0), AUX_W);
      wo1 = __builtin_amdgcn_raw_buffer_load_b64(ro, wo_vo + 1024, WO_SOFF(0), AUX_W);
#pragma unroll
      for (int n = NVE; n < NV0; ++n)
#pragma unroll
        for (int u = 0; u < 4; ++u) v0[n][u] = LDKV(rv, u, wave * 4096 + n * (NSW * 4096));
      SB();
      __syncthreads();
      float qa[8];
      const float* q0 = lds + L_QP + (2 * r) * 128 + 8 * c;
      const f32x4 x0 = *(const f32x4*)(q0), x1 = *(const f32x4*)(q0 + 4), y0 = *(const f32x4*)(q0 + 128), y1 = *(const f32x4*)(q0 + 132);
#pragma unroll
      for (int k = 0; k < 4; ++k) { qa[k] = xrow_sum(x0[k] + y0[k]); qa[k + 4] = xrow_sum(x1[k] + y1[k]); }
#pragma unroll
      for (int k = 0; k < 4; ++k) qp[k] = pack_bf16(qa[2 * k], qa[2 * k + 1]);
    }
    unsigned pstep = gi < NGLU ? PACE_K * 60 / 64 : PACE_K;
    unsigned due = (unsigned)t_start * 16u + T0 + (8 * (2 + NV0) + wave) * pstep;
    auto pace = [&]() {
      while ((int)((unsigned)__builtin_amdgcn_s_memrealtime() * 16u - due) < 0) __builtin_amdgcn_s_sleep(1);
      due += 8 * pstep;
    };
    float m;
    f32x4* sl = (f32x4*)(lds + L_S) + wave * 64 + r;
    {
      f32x4 sc;
#pragma unroll
      for (int u = 0; u < 4; ++u) sc[u] = score_of(row_allsum(qk_dot(pk[u], qp)));
      m = fmaxf(fmaxf(sc[0], sc[1]), fmaxf(sc[2], sc[3]));
      if (c == 0) sl[0] = sc;
    }
    unsigned* wo_lds = ldsu + L_WO + wave * 4096 + lane * 4;
#pragma unroll 1
    for (int it = 1; it < NITEM; ++it) {
      soff += NSW * 4096;
      const __amdgpu_buffer_rsrc_t rn = it + 1 < NITEM ? rk : rv;
      const unsigned sn = it + 1 < NITEM ? soff : (unsigned)(wave * 4096 + NV0 * NSW * 4096);
      SB();
      f32x4 sc;
#pragma unroll
      for (int u = 0; u < 4; ++u) sc[u] = qk_dot(a[u], qp);
      asm volatile("" : "+v"(sc));
      SB();
      pace();
      SB();
#pragma unroll
      for (int u = 0; u < 4; ++u) a[u] = LDKV(rn, u, sn);
      SB();
#pragma unroll
      for (int u = 0; u < 4; ++u) sc[u] = score_of(row_allsum(sc[u]));
      m = fmaxf(fmaxf(fmaxf(sc[0], sc[1]), fmaxf(sc[2], sc[3])), m);
      if (c == 0) sl[it * 4] = sc;
      SB();
      WO_STAGE(it - 1);
      SB();
      wo0 = __builtin_amdgcn_raw_buffer_load_b64(ro, wo_vo, WO_SOFF(it), AUX_W);
      wo1 = __builtin_amdgcn_raw_buffer_load_b64(ro, wo_vo + 1024, WO_SOFF(it), AUX_W);
      SB();
    }
    m = xrow_max(m);
    {
      f32x4 sc = sl[c * 4];
#pragma unroll
      for (int u = 0; u < 4; ++u) sc[u] = __builtin_amdgcn_exp2f(sc[u] - m);
      sl[c * 4] = sc;
      const float lw = xrow_sum(row_allsum((sc[0] + sc[1]) + (sc[2] + sc[3])));
      if (lane == 0) {
        *(f32x2*)(lds + L_ML + 2 * wave) = f32x2{m, lw};
        __hip_atomic_fetch_add((LAS unsigned*)ldsu + L_CTL + 9, 1u, __ATOMIC_RELEASE, __HIP_MEMORY_SCOPE_WORKGROUP);
      }
    }
    pstep = gi < NGLU ? PACE_V * 60 / 64 : PACE_V;
#pragma unroll
    for (int k = 0; k < 8; ++k) acc[k] = 0.f;
#pragma unroll
    for (int n = 0; n < NV0; ++n) {
      const f32x4 pr = sl[n * 4];
#pragma unroll
      for (int u = 0; u < 4; ++u) pv_step(acc, pr[u], v0[n][u]);
      PIN8(acc); SB();
    }
    WO_STAGE(NITEM - 1);
#pragma unroll
    for (int i = 0; i < 16; ++i) af[i] = *(const u32x4*)(wo_lds + i * 256);
    u32x4 wdr[4];
    {
      const __amdgpu_buffer_rsrc_t rd = __builtin_amdgcn_make_buffer_rsrc((void*)(p.wd + (size_t)(min(gi, NGLU - 1) * 32 + 4 * wave) * DM), 0, gi < NGLU ? 4 * DM * 2 : 0, 0x00020000);
#pragma unroll
      for (int i = 0; i < 4; ++i) wdr[i] = __builtin_amdgcn_raw_buffer_load_b128(rd, lo16, i * 1024, AUX_NT);
    }
    soff = wave * 4096 + NV0 * NSW * 4096;
#pragma unroll 1
    for (int it = NV0; it < NITEM - 1; ++it) {
      const f32x4 pr = sl[it * 4];
      soff += NSW * 4096;
      SB();
#pragma unroll
      for (int u = 0; u < 4; ++u) pv_step(acc, pr[u], a[u]);
      PIN8(acc); SB();
      pace();
      SB();
#pragma unroll
      for (int u = 0; u < 4; ++u) a[u] = LDKV(rv, u, soff);
      SB();
    }
    while (__hip_atomic_load((LAS unsigned*)ldsu + L_CTL + 10, __ATOMIC_ACQUIRE, __HIP_MEMORY_SCOPE_WORKGROUP) == 0u) __builtin_amdgcn_s_sleep(2);
    const float sw = lds[L_SC + wave], gate = lds[L_CTL + 8];
    const unsigned tag = __builtin_amdgcn_readfirstlane(ldsu[L_CTL]);
    const int n = 64 * wave + 16 * r + 4 * (c & 3) + (c >> 2);
    gu64* ydst = (gu64*)(p.ws + WS_YA + y_off(b, n, pidx));
    asm volatile("" : "+v"(ydst));
    const size_t g1off = WS_G1 + ((size_t)((gi * 4 + r) * 4 + (c & 3)) * 64 + 4 * wave + (c >> 2)) * 8;
    unsigned long long gg = 0, gu = 0;
    if (gi < NGLU) { gg = load_granule(p.ws, g1off); gu = load_granule(p.ws, g1off + 256); }
    SB();
    {
      const f32x4 pr = sl[(NITEM - 1) * 4];
#pragma unroll
      for (int u = 0; u < 4; ++u) pv_step(acc, pr[u], a[u]);
    }
    float hv = 0.f;
    if (gi < NGLU) {
      const unsigned tag0 = ldsu[L_CTL + 4];
#pragma unroll 1
      for (unsigned spins = 0; spins < SPIN_CAP; ++spins) {
        if (__all((unsigned)(gg >> 32) == tag0 && (unsigned)(gu >> 32) == tag0)) break;
        __builtin_amdgcn_s_sleep(4);
        gg = load_granule(p.ws, g1off); gu = load_granule(p.ws, g1off + 256);
      }
      const float gs = xrow_sum(__uint_as_float((unsigned)gg)), us = xrow_sum(__uint_as_float((unsigned)gu));
      hv = rbf(rbf(sigmoidf_(rbf(gs))) * rbf(us));
    }
    {
      const f32x2 t = xrow_treduce8(acc) * sw;
      const unsigned hi = pack_bf16(t.x, t.y);
      const unsigned lo = pack_bf16(t.x - bf_lo(hi), t.y - bf_hi(hi));
      ldsu[L_PB + wave * 68 + 4 * c + r] = hi;
      ldsu[L_PB + (8 + wave) * 68 + 4 * c + r] = lo;
    }
    __syncthreads();
    {
      u32x4 bf[4];
#pragma unroll
      for (int ks = 0; ks < 4; ++ks) bf[ks] = *(const u32x4*)(ldsu + L_PB + c * 68 + 16 * ks + 4 * r);
      float x[16];
#pragma unroll
      for (int t4 = 0; t4 < 4; ++t4) {
        f32x4 d = {0.f, 0.f, 0.f, 0.f};
#pragma unroll
        for (int ks = 0; ks < 4; ++ks)
          d = __builtin_amdgcn_mfma_f32_16x16x32_bf16(__builtin_bit_cast(bf16x8, af[ks * 4 + t4]), __builtin_bit_cast(bf16x8, bf[ks]), d, 0, 0, 0);
#pragma unroll
        for (int g = 0; g < 4; ++g) x[4 * t4 + g] = d[g];
      }
#define TR_STEP(HALF, CTRL, BIT) _Pragma("unroll") for (int i = 0; i < (HALF); ++i) { \
        const float t1_ = x[i] + dpp<CTRL>(x[i]), t2_ = x[i + (HALF)] + dpp<CTRL>(x[i + (HALF)]); x[i] = (c & (BIT)) ? t2_ : t1_; }
      TR_STEP(8, ROR8, 8) TR_STEP(4, XOR7, 4) TR_STEP(2, XOR2, 2) TR_STEP(1, XOR1, 1)
#undef TR_STEP
      __hip_atomic_store(ydst, ((unsigned long long)tag << 32) | __float_as_uint(x[0]), RLX_AGENT);
    }
    if (gi < NGLU) {
      float* yp = lds + L_WO + wave * 4096 + 8 * lane;
      float wf[4][8];
#pragma unroll
      for (int i = 0; i < 4; ++i)
#pragma unroll
        for (int k = 0; k < 4; ++k) { wf[i][2 * k] = bf_lo(wdr[i][k]); wf[i][2 * k + 1] = bf_hi(wdr[i][k]); }
#pragma unroll 1
      for (int bb = 0; bb < 4; ++bb) {
        float y[8];
#pragma unroll
        for (int k = 0; k < 8; ++k) y[k] = 0.f;
#pragma unroll
        for (int i = 0; i < 4; ++i) {
          const float hb = __builtin_bit_cast(float, __builtin_amdgcn_readlane(__builtin_bit_cast(int, hv), 4 * i + bb));
#pragma unroll
          for (int k = 0; k < 8; ++k) y[k] = fmaf(hb, wf[i][k], y[k]);
        }
        *(f32x4*)(yp + bb * 512) = f32x4{y[0], y[1], y[2], y[3]};
        *(f32x4*)(yp + bb * 512 + 4) = f32x4{y[4], y[5], y[6], y[7]};
      }
      __syncthreads();
#pragma unroll 1
      for (int bb = 0; bb < 4; ++bb) {
        float y = 0.f;
#pragma unroll
        for (int w = 0; w < 8; ++w) y += lds[L_WO + w * 4096 + bb * 512 + tid];
        store_granule(p.ws, WS_YG + y_off(bb, tid, gi), ldsu[L_CTL + 4 + bb], y);
      }
    }
    if (wave == 0) {
      __builtin_amdgcn_s_sleep(24);
      const bool live5 = 40 + (lane >> 3) < NGLU;
      float a, g;
      bool ok;
#pragma unroll 1
      for (unsigned spins = 0;; ++spins) {
        unsigned long long ga[8], gl[6];
#pragma unroll
        for (int jj = 0; jj < 8; ++jj) ga[jj] = load_granule(p.ws, WS_YA + gbase + jj * 1024);
#pragma unroll
        for (int jj = 0; jj < 6; ++jj) gl[jj] = load_granule(p.ws, WS_YG + gbase + jj * 1024);
        unsigned bad = 0;
#pragma unroll
        for (int jj = 0; jj < 8; ++jj) bad |= (unsigned)(ga[jj] >> 32) ^ tag;
#pragma unroll
        for (int jj = 0; jj < 5; ++jj) bad |= (unsigned)(gl[jj] >> 32) ^ tag;
        bad |= live5 ? (unsigned)(gl[5] >> 32) ^ tag : 0u;
        a = ((__uint_as_float((unsigned)ga[0]) + __uint_as_float((unsigned)ga[1])) + (__uint_as_float((unsigned)ga[2]) + __uint_as_float((unsigned)ga[3])))
          + ((__uint_as_float((unsigned)ga[4]) + __uint_as_float((unsigned)ga[5])) + (__uint_as_float((unsigned)ga[6]) + __uint_as_float((unsigned)ga[7])));
        g = ((__uint_as_float((unsigned)gl[0]) + __uint_as_float((unsigned)gl[1])) + (__uint_as_float((unsigned)gl[2]) + __uint_as_float((unsigned)gl[3])))
          + (__uint_as_float((unsigned)gl[4]) + (live5 ? __uint_as_float((unsigned)gl[5]) : 0.f));
        ok = __all(bad == 0u);
        if (ok || spins >= SPIN_CAP) break;
        __builtin_amdgcn_s_sleep(3);
      }
      a += dpp<ROR8>(a); g += dpp<ROR8>(g);
      a = xrow_sum(a); g = xrow_sum(g);
      float o = rbf(gate * rbf(a)) + rbf(rbf(1.f - gate) * rbf(g));
      if (!ok || !(sw == sw)) o = __builtin_nanf("");
      if (lane < 8) p.out[b * DM + 8 * pidx + lane] = __builtin_bit_cast(unsigned short, (__bf16)o);
      if (pidx == 0 && lane == 0) __hip_atomic_store((gu32*)(p.ws + WS_E + b * 256), tag, RLX_AGENT);
    }
  } else {
    const unsigned tags = __hip_atomic_load((gu32*)(p.ws + WS_E + (lane & 3) * 256), RLX_AGENT) + 1u;
    if (lane == 0) { ldsu[L_CTL + 9] = 0u; ldsu[L_CTL + 10] = 0u; }
    __syncthreads();
    SB();
    const unsigned tag = __builtin_amdgcn_readlane(tags, b), tag0 = __builtin_amdgcn_readlane(tags, 0);
    if (lane == 0) ldsu[L_CTL] = tag;
    if (lane < 4) ldsu[L_CTL + 4 + lane] = tags;
    {
      float gate;
      const u32x4 xg = *(const u32x4*)(p.x + b * DM + 8 * lane), gw = *(const u32x4*)(p.gw + 8 * lane);
      float z = 0.f;
#pragma unroll
      for (int k = 0; k < 4; ++k) z += rbf(bf_lo(xg[k]) * bf_lo(gw[k])) + rbf(bf_hi(xg[k]) * bf_hi(gw[k]));
      gate = rbf(sigmoidf_(rbf(xrow_sum(row_allsum(z)))));
      if (lane == 0) lds[L_CTL + 8] = gate;
    }
    const int k1 = j - 6;
    const unsigned t1 = (k1 >= 0 && k1 < ((grp & 1) ? 21 : 22)) ? (unsigned)(((grp & 1) * 22 + k1) * 4 + (grp >> 1)) : 9999u;
    if (t1 < 4u * NGLU) {
      const int cg = t1 >> 2, rb = t1 & 3, row0 = 128 * rb + 16 * (lane >> 3);
      const __amdgpu_buffer_rsrc_t rg = __builtin_amdgcn_make_buffer_rsrc((void*)p.wg, 0, DM * FF * 2, 0x00020000);
      const __amdgpu_buffer_rsrc_t ru = __builtin_amdgcn_make_buffer_rsrc((void*)p.wu, 0, DM * FF * 2, 0x00020000);
      const unsigned vo = (row0 * FF + 32 * cg + 4 * (lane & 7)) * 2;
      u32x4 xr[4][2];
#pragma unroll
      for (int bb = 0; bb < 4; ++bb) { xr[bb][0] = *(const u32x4*)(p.x + bb * DM + row0); xr[bb][1] = *(const u32x4*)(p.x + bb * DM + row0 + 8); }
      u32x2 wgr[16], wur[16];
      unsigned gdue = (unsigned)t_start + G1_T0;
#pragma unroll
      for (int i = 0; i < 16; ++i) {
        if ((i & 1) == 0) { while ((int)((unsigned)__builtin_amdgcn_s_memrealtime() - gdue) < 0) __builtin_amdgcn_s_sleep(4); gdue += G1_GAP; SB(); }
        wgr[i] = __builtin_amdgcn_raw_buffer_load_b64(rg, vo, i * (FF * 2), AUX_W); wur[i] = __builtin_amdgcn_raw_buffer_load_b64(ru, vo, i * (FF * 2), AUX_W);
        SB();
      }
#pragma unroll 1
      for (int bb = 0; bb < 4; ++bb) {
        const u32x4 x0 = bb == 0 ? xr[0][0] : bb == 1 ? xr[1][0] : bb == 2 ? xr[2][0] : xr[3][0];
        const u32x4 x1 = bb == 0 ? xr[0][1] : bb == 1 ? xr[1][1] : bb == 2 ? xr[2][1] : xr[3][1];
        float ag[4] = {0.f, 0.f, 0.f, 0.f}, au[4] = {0.f, 0.f, 0.f, 0.f};
#pragma unroll
        for (int i = 0; i < 16; ++i) {
          const unsigned d = i < 8 ? x0[(i >> 1) & 3] : x1[(i >> 1) & 3];
          const unsigned xl = (i & 1) ? (d >> 16) : (d & 0xffffu), xh = (i & 1) ? (d & 0xffff0000u) : (d << 16);
#pragma unroll
          for (int k = 0; k < 2; ++k) {
            ag[2 * k] = dot2(wgr[i][k], xl, ag[2 * k]); ag[2 * k + 1] = dot2(wgr[i][k], xh, ag[2 * k + 1]);
            au[2 * k] = dot2(wur[i][k], xl, au[2 * k]); au[2 * k + 1] = dot2(wur[i][k], xh, au[2 * k + 1]);
          }
        }
        float tg = swap16_sum(swap32_sum(ag[0], ag[2]), swap32_sum(ag[1], ag[3]));
        float tu = swap16_sum(swap32_sum(au[0], au[2]), swap32_sum(au[1], au[3]));
        tg += dpp<ROR8>(tg); tu += dpp<ROR8>(tu);
        store_granule(p.ws, WS_G1 + ((size_t)((cg * 4 + rb) * 4 + bb) * 64 + ((lane >> 3) & 1) * 32 + 4 * (lane & 7) + r) * 8, tag0, (lane & 8) ? tu : tg);
      }
    }
    while (__hip_atomic_load((LAS unsigned*)ldsu + L_CTL + 9, __ATOMIC_ACQUIRE, __HIP_MEMORY_SCOPE_WORKGROUP) < (unsigned)NSW) __builtin_amdgcn_s_sleep(8);
    const f32x2 ml = *(const f32x2*)(lds + L_ML + 2 * (lane & 7));
    float M0 = fmaxf(ml.x, dpp<XOR1>(ml.x)); M0 = fmaxf(M0, dpp<XOR2>(M0)); M0 = fmaxf(M0, dpp<XOR7>(M0));
    float L0 = __builtin_amdgcn_exp2f(ml.x - M0) * ml.y; L0 += dpp<XOR1>(L0); L0 += dpp<XOR2>(L0); L0 += dpp<XOR7>(L0);
    if (lane < 2) store_granule(p.ws, WS_ML + ((size_t)(b * 64 + pidx) * 2 + lane) * 8, tag, lane ? L0 : M0);
    {
      unsigned long long gp;
      for (unsigned spins = 0;; ++spins) {
        gp = load_granule(p.ws, WS_ML + ((size_t)(b * 64 + (pidx ^ 1)) * 2 + (lane & 1)) * 8);
        if (__all((unsigned)(gp >> 32) == tag)) break;
        if (spins >= SPIN_CAP) { gp = 0x7fc00000u; break; }
        __builtin_amdgcn_s_sleep(8);
      }
      const float v = __uint_as_float((unsigned)gp);
      const float M1 = dpp<0xA0>(v), L1 = dpp<0xF5>(v);
      const float M = fmaxf(M0, M1);
      const float L = fmaf(__builtin_amdgcn_exp2f(M0 - M), L0, __builtin_amdgcn_exp2f(M1 - M) * L1);
      if (lane < 8) lds[L_SC + lane] = __builtin_amdgcn_exp2f(ml.x - M) / L;
    }
    if (lane == 0) __hip_atomic_store((LAS unsigned*)ldsu + L_CTL + 10, 1u, __ATOMIC_RELEASE, __HIP_MEMORY_SCOPE_WORKGROUP);
    __syncthreads();
    if (gi < NGLU) __syncthreads();
  }
}

}

extern "C" void kernel_launch(void* const* d_in, const int* in_sizes, int n_in, void* d_out, int out_size, void* d_ws,
                              size_t ws_size, hipStream_t stream) {
  (void)in_sizes; (void)n_in; (void)out_size; (void)ws_size;
  Params p;
  p.x = (const unsigned short*)d_in[0]; p.k = (const unsigned short*)d_in[1]; p.v = (const unsigned short*)d_in[2];
  p.wq = (const unsigned short*)d_in[3]; p.wo = (const unsigned short*)d_in[4];
  p.wg = (const unsigned short*)d_in[6]; p.wu = (const unsigned short*)d_in[7]; p.wd = (const unsigned short*)d_in[8];
  p.gw = (const unsigned short*)d_in[9];
  p.out = (unsigned short*)d_out; p.ws = (unsigned char*)d_ws;
  hipLaunchKernelGGL(fused_decode_kernel, dim3(NWG), dim3(NTHR), 0, stream, p);
}
```
